```python
import torch

def _softmax(x, axis=-1):
    return torch.softmax(x, dim=axis)
def _gelu(x):
    return torch.nn.functional.gelu(x, approximate="tanh")
def _silu(x):
    return torch.nn.functional.silu(x)
def _sigmoid(x):
    return torch.sigmoid(x)
def _layernorm(x, axis=-1, eps=1e-5):
    m = x.mean(dim=axis, keepdim=True)
    v = x.var(dim=axis, keepdim=True, unbiased=False)
    return (x - m) / torch.sqrt(v + eps)
def _scatter_add(tgt, idx, src, axis=0):
    return tgt.clone().index_add_(axis, idx, src)
def _segment_sum(src, seg, n):
    out = torch.zeros((n,) + src.shape[1:], dtype=src.dtype)
    return out.index_add_(0, seg, src)
def _pad(x, pw):
    flat = tuple(v for pair in reversed(pw) for v in pair)
    return torch.nn.functional.pad(x, flat)

kernel_name = 'interleave_layers__sliding_block__glu_gated_glu__prompt'

def setup_inputs():

    _w = (lambda a: (lambda t: t.to(torch.bfloat16) if t.is_floating_point() else t)(torch.as_tensor(a)))
    def _A_setup():
        B, S, D = 1, 2048, 640
        return {
            "x": _w((lambda *s: torch.randn(*s, dtype=torch.bfloat16) * 0.05)(B, S, D)),
            "wqkv": _w((lambda *s: torch.randn(*s, dtype=torch.bfloat16) * 0.05)(D, 3 * D)),
            "wo": _w((lambda *s: torch.randn(*s, dtype=torch.bfloat16) * 0.05)(D, D)),
        }
    _A_in = _A_setup()

    def _B_setup():
        B, S, D, F = 1, 2048, 640, 1536
        return {
            "x": _w((lambda *s: torch.randn(*s, dtype=torch.bfloat16) * 0.05)(B, S, D)),
            "w_gate": _w((lambda *s: torch.randn(*s, dtype=torch.bfloat16) * 0.05)(D, F)),
            "w_up": _w((lambda *s: torch.randn(*s, dtype=torch.bfloat16) * 0.05)(D, F)),
            "w_down": _w((lambda *s: torch.randn(*s, dtype=torch.bfloat16) * 0.05)(F, D)),
        }
    _B_in = _B_setup()

    return {
        "A_x": _A_in["x"],
        "A_wqkv": _A_in["wqkv"],
        "A_wo": _A_in["wo"],
        "B_x": _B_in["x"],
        "B_w_gate": _B_in["w_gate"],
        "B_w_up": _B_in["w_up"],
        "B_w_down": _B_in["w_down"],
    }

def reference(A_x, A_wqkv, A_wo, B_x, B_w_gate, B_w_up, B_w_down):
    def _A_ref(x, wqkv, wo):
        B, S, D = x.shape
        H = 32; Dh = D // H; W = 256
        qkv = (lambda x, *d: x.permute(*d))((x @ wqkv).reshape(B, S, 3, H, Dh), 2, 0, 3, 1, 4)
        q, k, v = qkv[0], qkv[1], qkv[2]
        scores = torch.einsum("bhqd,bhkd->bhqk", q, k) * (Dh ** -0.5)
        idx = torch.arange(S)
        mask = (idx[:, None] - idx[None, :] >= 0) & (idx[:, None] - idx[None, :] < W)
        scores = torch.where(mask[None, None], scores, -1e9)
        p = _softmax(scores, axis=-1)
        o = torch.einsum("bhqk,bhkd->bhqd", p, v)
        return (lambda x, *d: x.permute(*d))(o, 0, 2, 1, 3).reshape(B, S, D) @ wo

    def _B_ref(x, w_gate, w_up, w_down):
        g = torch.einsum("bsd,df->bsf", x, w_gate)
        u = torch.einsum("bsd,df->bsf", x, w_up)
        h = _sigmoid(g) * u
        return torch.einsum("bsf,fd->bsd", h, w_down)

    x = A_x
    x = x + _A_ref(_layernorm(x, axis=-1), A_wqkv, A_wo)
    x = x + _B_ref(_layernorm(x, axis=-1), B_w_gate, B_w_up, B_w_down)
    x = x + _A_ref(_layernorm(x, axis=-1), A_wqkv, A_wo)
    x = x + _B_ref(_layernorm(x, axis=-1), B_w_gate, B_w_up, B_w_down)
    return x
```

```cpp
#include <hip/hip_runtime.h>
#include <hip/hip_bf16.h>
typedef __hip_bfloat16 bf16;
static __device__ __forceinline__ float b2f(bf16 v){ return __bfloat162float(v);}
static __device__ __forceinline__ bf16 f2b(float v){ return __float2bfloat16(v);}
static __device__ __forceinline__ float rb(float v){ return b2f(f2b(v)); }
constexpr int S=2048,D=640,H=32,DH=20,W=256,F=1536;

__global__ void ln_k(const bf16* x, bf16* y){
  int row=blockIdx.x; int t=threadIdx.x;
  float v[10]; float s=0;
  for(int i=0;i<10;i++){ v[i]=b2f(x[row*D+t+64*i]); s+=v[i]; }
  for(int m=32;m>=1;m>>=1) s+=__shfl_xor(s,m);
  float mean=s/D; float q=0;
  for(int i=0;i<10;i++){ float d=v[i]-mean; q+=d*d; }
  for(int m=32;m>=1;m>>=1) q+=__shfl_xor(q,m);
  float var=q/D;
  float mb=mean, vb=var;
  float sd=sqrtf(vb+1e-5f);
  for(int i=0;i<10;i++){ y[row*D+t+64*i]=f2b((v[i]-mb)/sd); }
}
template<int MODE>
__global__ void gemm_k(const bf16* A,const bf16* B,bf16* C,const bf16* res,int M,int N,int K){
  __shared__ float sa[16][17], sb[16][17];
  int tx=threadIdx.x, ty=threadIdx.y; int row=blockIdx.y*16+ty, col=blockIdx.x*16+tx;
  float acc=0;
  for(int k0=0;k0<K;k0+=16){
    sa[ty][tx]=b2f(A[row*K+k0+tx]); sb[ty][tx]=b2f(B[(k0+ty)*N+col]);
    __syncthreads();
    for(int k=0;k<16;k++) acc+=sa[ty][k]*sb[k][tx];
    __syncthreads();
  }
  if(MODE==0) C[row*N+col]=f2b(acc);
  else C[row*N+col]=f2b(b2f(res[row*N+col])+rb(acc));
}
__global__ void glu_k(const bf16* g,const bf16* u,bf16* h,int n){
  int i=blockIdx.x*256+threadIdx.x; if(i>=n) return;
  float gg=b2f(g[i]); float sg=rb(1.f/(1.f+__expf(-gg)));
  h[i]=f2b(sg*b2f(u[i]));
}
__global__ void attn_k(const bf16* qkv, bf16* o){
  int i=blockIdx.x, h=blockIdx.y, t=threadIdx.x;
  __shared__ float p[256]; __shared__ float red[4];
  int j=i-t; float s=-1e30f;
  if(j>=0){ float a=0; for(int d=0;d<DH;d++) a+=b2f(qkv[i*3*D+h*DH+d])*b2f(qkv[j*3*D+D+h*DH+d]); s=a*0.22360679775f; }
  float m=s; for(int k=32;k>=1;k>>=1) m=fmaxf(m,__shfl_xor(m,k));
  if((t&63)==0) red[t>>6]=m; __syncthreads();
  m=fmaxf(fmaxf(red[0],red[1]),fmaxf(red[2],red[3])); __syncthreads();
  float e=(j>=0)?__expf(s-m):0.f; float z=e; for(int k=32;k>=1;k>>=1) z+=__shfl_xor(z,k);
  if((t&63)==0) red[t>>6]=z; __syncthreads();
  z=red[0]+red[1]+red[2]+red[3];
  p[t]=rb(e/z); __syncthreads();
  if(t<DH){ float a=0; for(int k=0;k<256;k++){ int jj=i-k; if(jj>=0) a+=p[k]*b2f(qkv[jj*3*D+2*D+h*DH+t]); } o[i*D+h*DH+t]=f2b(a); }
}
extern "C" void kernel_launch(void* const* d_in, const int* in_sizes, int n_in, void* d_out, int out_size, void* d_ws, size_t ws_size, hipStream_t stream){
  const bf16* x=(const bf16*)d_in[0]; const bf16* wqkv=(const bf16*)d_in[1]; const bf16* wo=(const bf16*)d_in[2];
  const bf16* wg=(const bf16*)d_in[4]; const bf16* wu=(const bf16*)d_in[5]; const bf16* wd=(const bf16*)d_in[6];
  bf16* ws=(bf16*)d_ws; bf16* xn=ws; bf16* qkv=xn+S*D; bf16* o=qkv+S*3*D; bf16* x1=o+S*D; bf16* g=x1+S*D; bf16* u=g+S*F; bf16* hh=u+S*F; bf16* x2=hh+S*F;
  bf16* out=(bf16*)d_out;
  const bf16* cur=x;
  for(int l=0;l<2;l++){
    ln_k<<<S,64,0,stream>>>(cur,xn);
    gemm_k<0><<<dim3(3*D/16,S/16),dim3(16,16),0,stream>>>(xn,wqkv,qkv,nullptr,S,3*D,D);
    attn_k<<<dim3(S,H),256,0,stream>>>(qkv,o);
    gemm_k<1><<<dim3(D/16,S/16),dim3(16,16),0,stream>>>(o,wo,x1,cur,S,D,D);
    ln_k<<<S,64,0,stream>>>(x1,xn);
    gemm_k<0><<<dim3(F/16,S/16),dim3(16,16),0,stream>>>(xn,wg,g,nullptr,S,F,D);
    gemm_k<0><<<dim3(F/16,S/16),dim3(16,16),0,stream>>>(xn,wu,u,nullptr,S,F,D);
    glu_k<<<S*F/256,256,0,stream>>>(g,u,hh,S*F);
    bf16* dst=(l==0)?x2:out;
    gemm_k<1><<<dim3(D/16,S/16),dim3(16,16),0,stream>>>(hh,wd,dst,x1,S,D,F);
    cur=x2;
  }
}
```

```cpp
#include <hip/hip_runtime.h>
#include <hip/hip_bf16.h>
#include <stdint.h>

typedef __bf16 bf16x8 __attribute__((ext_vector_type(8)));
typedef __bf16 bf16x2 __attribute__((ext_vector_type(2)));
typedef short s16x4 __attribute__((ext_vector_type(4)));
typedef float f32x4 __attribute__((ext_vector_type(4)));
typedef float f32x2 __attribute__((ext_vector_type(2)));
typedef unsigned u32x4 __attribute__((ext_vector_type(4)));
typedef unsigned u32x2 __attribute__((ext_vector_type(2)));
typedef unsigned short u16;
#define LAS __attribute__((address_space(3)))
#define DEV __device__ __forceinline__

constexpr int S_ = 2048, D_ = 640, H_ = 32, F_ = 1536;

DEV f32x4 mfma16(u32x4 a, u32x4 b, f32x4 c) {
  return __builtin_amdgcn_mfma_f32_16x16x32_bf16(__builtin_bit_cast(bf16x8, a), __builtin_bit_cast(bf16x8, b), c, 0, 0, 0);
}
DEV float dot2(unsigned a, unsigned b, float c) {
#if defined(__HIP_DEVICE_COMPILE__)
  return __builtin_amdgcn_fdot2_f32_bf16(__builtin_bit_cast(bf16x2, a), __builtin_bit_cast(bf16x2, b), c, false);
#else
  return 0.f;
#endif
}
DEV unsigned pack2(float lo, float hi) {
  f32x2 p = {lo, hi};
  bf16x2 q = __builtin_convertvector(p, bf16x2);
  return __builtin_bit_cast(unsigned, q);
}
DEV float bf_lo(unsigned u) { return __builtin_bit_cast(float, u << 16); }
DEV float bf_hi(unsigned u) { return __builtin_bit_cast(float, u & 0xffff0000u); }
DEV float rbf(float x) { return bf_lo(pack2(x, 0.f)); }
DEV u32x2 tr_read(const LAS char* p) {
#if defined(__HIP_DEVICE_COMPILE__)
  s16x4 v = __builtin_amdgcn_ds_read_tr16_b64_v4i16((LAS s16x4*)p);
  return __builtin_bit_cast(u32x2, v);
#else
  return u32x2{0, 0};
#endif
}
template <int OFF = 0> DEV void glds16(const void* g, LAS char* l) {
  unsigned keep;
  const unsigned m = (unsigned)(uintptr_t)l - OFF;
  asm volatile("s_mov_b32 %0, m0\n\ts_mov_b32 m0, %2\n\ts_nop 0\n\tglobal_load_lds_dwordx4 %1, off offset:%3\n\ts_mov_b32 m0, %0"
               : "=&s"(keep) : "v"(g), "s"(m), "n"(OFF) : "memory");
}
template <int N> DEV void wait_vm() { asm volatile("s_waitcnt vmcnt(%0)" ::"n"(N) : "memory"); }
DEV void wait_lgkm0() { asm volatile("s_waitcnt lgkmcnt(0)" ::: "memory"); }
DEV void bar() { asm volatile("s_waitcnt lgkmcnt(0)" ::: "memory"); __builtin_amdgcn_s_barrier(); asm volatile("" ::: "memory"); }
DEV u32x4 lds_b128(const LAS char* p) { return *(const LAS u32x4*)p; }
DEV u32x2 lds_b64(const LAS char* p) { return *(const LAS u32x2*)p; }

template <int MODE>
__global__ __launch_bounds__(512, 2) void gemm_ln(const u16* __restrict__ act, const u16* __restrict__ W0,
                                                  const u16* __restrict__ W1, u16* __restrict__ o0,
                                                  u16* __restrict__ o1, u16* __restrict__ o2,
                                                  u16* __restrict__ o3) {
  constexpr int NCP = (MODE == 2) ? 3 : 2;
  constexpr int SLOT = 16384 + 8 * NCP * 1024;
  constexpr int NSLOT = (MODE == 2) ? 4 : 5;
  constexpr int PPC = 2 + NCP;
  constexpr int NFT = (MODE == 2) ? 6 : 4;
  constexpr int NF = (MODE == 2) ? 192 : 128;
  constexpr int LDW = (MODE == 2) ? F_ : 3 * D_;
  __shared__ __attribute__((aligned(16))) char smem_[SLOT * NSLOT];
  LAS char* smem = (LAS char*)smem_;

  const int b = blockIdx.x, x8 = b & 7, bi = b >> 3;
  int rb, cb;
  if (MODE == 2) { rb = 8 * (x8 >> 2) + (bi & 7); cb = 4 * (x8 & 3) + (bi >> 3); }
  else { rb = 4 * (x8 >> 1) + (bi & 3); cb = 8 * (x8 & 1) + (bi >> 2); if (cb >= 15) return; }

  const int tid = threadIdx.x, lane = tid & 63;
  const int w = __builtin_amdgcn_readfirstlane(tid >> 6);
  const int tg = w & 3, fh = w >> 2;
  const int l15 = lane & 15, q = lane >> 4;

  const u16* asrc0; const u16* asrc1;
  if (MODE == 0) {
    const int r = lane >> 3, cp_ = lane & 7;
    asrc0 = act + (size_t)(rb * 128 + 16 * w + r) * D_ + 8 * (cp_ ^ (r >> 1));
    asrc1 = act + (size_t)(rb * 128 + 16 * w + 8 + r) * D_ + 8 * (cp_ ^ (4 + (r >> 1)));
  } else {
    asrc0 = act + ((size_t)(rb * 8 + w) * 20 * 64 + lane) * 8;
    asrc1 = asrc0 + 512;
  }
  constexpr int ASTEP = (MODE == 0) ? 64 : 1024;
  const u16* wsrc;
  {
    const int r = lane >> 3, cp_ = lane & 7, c = cp_ ^ (((r >> 1) & 3) << 1);
    if (MODE == 2) wsrc = ((c < 4) ? W0 : W1) + (size_t)(8 * w + r) * LDW + 96 * cb + 8 * (c & 3);
    else wsrc = W0 + (size_t)(8 * w + r) * LDW + 128 * cb + 8 * c;
  }
  constexpr int WCP = (MODE == 2) ? 32 : 64;

  auto issue = [&](int slot_off) {
    LAS char* s = smem + slot_off;
    glds16(asrc0, s + w * 2048);
    if (MODE == 0) glds16(asrc1, s + w * 2048 + 1024); else glds16<1024>(asrc0, s + w * 2048 + 1024);
    glds16<0>(wsrc, s + 16384 + (w * NCP) * 1024);
    glds16<WCP * 2>(wsrc, s + 16384 + (w * NCP + 1) * 1024);
    if (NCP == 3) glds16<WCP * 4>(wsrc, s + 16384 + (w * NCP + 2) * 1024);
    asrc0 += ASTEP; asrc1 += ASTEP; wsrc += 64 * LDW;
  };
#pragma unroll
  for (int c = 0; c < NSLOT - 1; ++c) issue(c * SLOT);

  int woff[4];
  {
    const int qq = (lane & 15) >> 2, p = lane & 3, r = 4 * (q & 1) + qq, swz = ((r >> 1) & 3) << 1;
#pragma unroll
    for (int ft = 0; ft < 4; ++ft)
      woff[ft] = 16384 + (q >> 1) * NCP * 1024 + r * 128 + (((2 * ft) ^ swz) + (p >> 1)) * 16 + (p & 1) * 8;
  }
  int aoff[4];
  if (MODE == 0) {
    const int s = (l15 >> 1) & 7;
#pragma unroll
    for (int i = 0; i < 4; ++i) aoff[i] = tg * 4096 + l15 * 128 + (((2 * i + (q >> 1)) ^ s) * 16) + (q & 1) * 8;
  } else {
    aoff[0] = tg * 4096 + lane * 16;
  }

  f32x4 acc[2][NFT];
#pragma unroll
  for (int i = 0; i < 2; ++i)
#pragma unroll
    for (int t = 0; t < NFT; ++t) acc[i][t] = f32x4{0.f, 0.f, 0.f, 0.f};
  float sx = 0.f, sxx = 0.f;
  float cs[NFT];
#pragma unroll
  for (int t = 0; t < NFT; ++t) cs[t] = 0.f;
  const bool vblk = (MODE != 2) && (cb >= 10);

  int cur = 0, nxt = (NSLOT - 1) * SLOT;
#pragma unroll 1
  for (int c = 0; c < 10; ++c) {
    {
      const int after = (9 - c) < (NSLOT - 2) ? (9 - c) : (NSLOT - 2);
      if (after == NSLOT - 2) wait_vm<(NSLOT - 2) * PPC>();
      else if (after == 2) wait_vm<2 * PPC>();
      else if (after == 1) wait_vm<PPC>();
      else wait_vm<0>();
    }
    bar();
    if (c + NSLOT - 1 < 10) issue(nxt);
    const LAS char* s = smem + cur;
    nxt = cur;
    cur += SLOT; if (cur == NSLOT * SLOT) cur = 0;
    const bool docs = ((c & 3) == tg);
#pragma unroll
    for (int ks = 0; ks < 2; ++ks) {
      u32x4 a[2];
#pragma unroll
      for (int i = 0; i < 2; ++i) {
        if (MODE == 0) {
          u32x2 lo = lds_b64(s + i * 2048 + aoff[2 * ks]);
          u32x2 hi = lds_b64(s + i * 2048 + aoff[2 * ks + 1]);
          a[i] = u32x4{lo.x, lo.y, hi.x, hi.y};
        } else {
          a[i] = lds_b128(s + aoff[0] + i * 2048 + ks * 1024);
        }
      }
      u32x4 wf[NFT];
#pragma unroll
      for (int t = 0; t < NFT; ++t) {
        int cp, ft;
        if (MODE == 2) {
          cp = 0; ft = 0;
        } else { cp = 0; ft = t; }
        (void)cp; (void)ft;
      }
      if (MODE == 2) {
#pragma unroll
        for (int t = 0; t < 3; ++t) {
          const int T0 = t, T1 = 3 + t;
          const int og = fh ? (woff[T1 & 1] + (T1 >> 1) * 1024) : (woff[T0 & 1] + (T0 >> 1) * 1024);
          const int ou = fh ? (woff[2 + (T1 & 1)] + (T1 >> 1) * 1024) : (woff[2 + (T0 & 1)] + (T0 >> 1) * 1024);
          u32x2 g0 = tr_read(s + og + (4 * ks) * NCP * 1024);
          u32x2 g1 = tr_read(s + og + (4 * ks + 2) * NCP * 1024);
          u32x2 u0 = tr_read(s + ou + (4 * ks) * NCP * 1024);
          u32x2 u1 = tr_read(s + ou + (4 * ks + 2) * NCP * 1024);
          wf[t] = u32x4{g0.x, g0.y, g1.x, g1.y};
          wf[3 + t] = u32x4{u0.x, u0.y, u1.x, u1.y};
        }
      } else {
#pragma unroll
        for (int t = 0; t < 4; ++t) {
          u32x2 g0 = tr_read(s + woff[t] + fh * 1024 + (4 * ks) * NCP * 1024);
          u32x2 g1 = tr_read(s + woff[t] + fh * 1024 + (4 * ks + 2) * NCP * 1024);
          wf[t] = u32x4{g0.x, g0.y, g1.x, g1.y};
        }
      }
      if (vblk) {
#pragma unroll
        for (int i = 0; i < 2; ++i)
#pragma unroll
          for (int t = 0; t < NFT; ++t) acc[i][t] = mfma16(a[i], wf[t], acc[i][t]);
      } else {
#pragma unroll
        for (int i = 0; i < 2; ++i)
#pragma unroll
          for (int t = 0; t < NFT; ++t) acc[i][t] = mfma16(wf[t], a[i], acc[i][t]);
      }
      {
        const u32x4 am = fh ? a[1] : a[0];
        const unsigned e0 = am.x, e1 = am.y, e2 = am.z, e3 = am.w;
        sx = dot2(e0, 0x3f803f80u, sx); sxx = dot2(e0, e0, sxx);
        sx = dot2(e1, 0x3f803f80u, sx); sxx = dot2(e1, e1, sxx);
        sx = dot2(e2, 0x3f803f80u, sx); sxx = dot2(e2, e2, sxx);
        sx = dot2(e3, 0x3f803f80u, sx); sxx = dot2(e3, e3, sxx);
      }
      if (docs) {
#pragma unroll
        for (int t = 0; t < NFT; ++t) {
          const unsigned e0 = wf[t].x, e1 = wf[t].y, e2 = wf[t].z, e3 = wf[t].w;
          cs[t] = dot2(e0, 0x3f803f80u, cs[t]); cs[t] = dot2(e1, 0x3f803f80u, cs[t]);
          cs[t] = dot2(e2, 0x3f803f80u, cs[t]); cs[t] = dot2(e3, 0x3f803f80u, cs[t]);
        }
      }
    }
  }
  LAS float* st = (LAS float*)(smem + (8 % NSLOT) * SLOT);
  LAS float* csp = st + 256;
  sx += __shfl_xor(sx, 16); sx += __shfl_xor(sx, 32);
  sxx += __shfl_xor(sxx, 16); sxx += __shfl_xor(sxx, 32);
  if (q == 0) {
    const float mean = sx * (1.f / D_);
    float var = fmaxf(sxx * (1.f / D_) - mean * mean, 0.f);
    const float mb = rbf(mean);
    const float sd = rbf(sqrtf(rbf(rbf(var) + 1e-5f)));
    const int tok = (2 * tg + fh) * 16 + l15;
    st[2 * tok] = mb; st[2 * tok + 1] = 1.f / sd;
  }
#pragma unroll
  for (int t = 0; t < NFT; ++t) {
    float v = cs[t];
    v += __shfl_xor(v, 16); v += __shfl_xor(v, 32);
    if (q == 0) csp[tg * NF + (fh * NFT + t) * 16 + l15] = v;
  }
  wait_lgkm0();
  bar();

  if (MODE == 2) {
    u16* h_t = o0;
#pragma unroll
    for (int i = 0; i < 2; ++i) {
      const int tok = (2 * tg + i) * 16 + l15;
      const float mb = st[2 * tok], rs = st[2 * tok + 1];
      const int TT = rb * 8 + 2 * tg + i;
#pragma unroll
      for (int t = 0; t < 3; ++t) {
        f32x4 cg = f32x4{0.f, 0.f, 0.f, 0.f}, cu = cg;
#pragma unroll
        for (int p = 0; p < 4; ++p) {
          cg += *(const LAS f32x4*)(csp + p * NF + (fh * 6 + t) * 16 + 4 * q);
          cu += *(const LAS f32x4*)(csp + p * NF + (fh * 6 + 3 + t) * 16 + 4 * q);
        }
        float hv[4];
#pragma unroll
        for (int r = 0; r < 4; ++r) {
          const float g = (acc[i][t][r] - mb * cg[r]) * rs;
          const float u = (acc[i][3 + t][r] - mb * cu[r]) * rs;
          hv[r] = u * __builtin_amdgcn_rcpf(1.f + __builtin_amdgcn_exp2f(-1.44269504089f * g));
        }
        const int T = 6 * cb + 3 * fh + t;
        u32x2 o = u32x2{pack2(hv[0], hv[1]), pack2(hv[2], hv[3])};
        *(u32x2*)(h_t + (((size_t)TT * 48 + (T >> 1)) * 64 + lane) * 8 + (T & 1) * 4) = o;
      }
    }
  } else if (!vblk) {
#pragma unroll
    for (int i = 0; i < 2; ++i) {
      const int tok = (2 * tg + i) * 16 + l15;
      const float mb = st[2 * tok], rs = st[2 * tok + 1];
      const int TT = rb * 8 + 2 * tg + i;
#pragma unroll
      for (int t = 0; t < 4; ++t) {
        f32x4 cg = f32x4{0.f, 0.f, 0.f, 0.f};
#pragma unroll
        for (int p = 0; p < 4; ++p) cg += *(const LAS f32x4*)(csp + p * NF + (fh * 4 + t) * 16 + 4 * q);
        float v[4];
#pragma unroll
        for (int r = 0; r < 4; ++r) v[r] = (acc[i][t][r] - mb * cg[r]) * rs;
        int g = 32 * cb + 16 * fh + 4 * t + q;
        u16* dst = o0;
        if (g >= 160) { g -= 160; dst = o1; }
        const int head = (g * 205) >> 10, dg = g - head * 5;
        u32x2 o = u32x2{pack2(v[0], v[1]), pack2(v[2], v[3])};
        *(u32x2*)(dst + (((size_t)head * 128 + TT) * 5 + dg) * 64 + l15 * 4) = o;
      }
    }
  } else {
    float mb[2][4], rs[2][4];
#pragma unroll
    for (int i = 0; i < 2; ++i)
#pragma unroll
      for (int r = 0; r < 4; ++r) {
        const int tok = (2 * tg + i) * 16 + 4 * q + r;
        mb[i][r] = st[2 * tok]; rs[i][r] = st[2 * tok + 1];
      }
    const int kg = rb * 4 + tg;
#pragma unroll
    for (int t = 0; t < 4; ++t) {
      float cg = 0.f;
#pragma unroll
      for (int p = 0; p < 4; ++p) cg += csp[p * NF + (fh * 4 + t) * 16 + l15];
      float v[2][4];
#pragma unroll
      for (int i = 0; i < 2; ++i)
#pragma unroll
        for (int r = 0; r < 4; ++r) v[i][r] = (acc[i][t][r] - mb[i][r] * cg) * rs[i][r];
      const int fv = 128 * (cb - 10) + 64 * fh + 16 * t + l15;
      const int head = (fv * 3277) >> 16, d = fv - head * 20;
      u32x4 o = u32x4{pack2(v[0][0], v[0][1]), pack2(v[0][2], v[0][3]), pack2(v[1][0], v[1][1]), pack2(v[1][2], v[1][3])};
      if (d < 16) *(u32x4*)(o2 + ((((size_t)head * 64 + kg) * 4 + q) * 16 + d) * 8) = o;
      else *(u32x4*)(o3 + ((((size_t)head * 64 + kg) * 4 + q) * 4 + (d - 16)) * 8) = o;
    }
  }
}

template <int KS, int RES_RM, int OUT_RM>
__global__ __launch_bounds__(512, 2) void gemm_res(const u16* __restrict__ act, const u16* __restrict__ W,
                                                   const u16* __restrict__ res, u16* __restrict__ out) {
  constexpr int KPW = KS / 4;
  constexpr int CK = (KS == 20) ? 5 : 2;
  constexpr int NCH = KPW / CK;
  constexpr int SLOT = 4 * CK * 6 * 1024;
  constexpr int NSLOT = (KS == 20) ? 1 : 3;
  constexpr int PPC = 4 * CK * 6 / 8;
  constexpr int RSTRIDE = 336;
  constexpr int RESOFF = 147456;
  __shared__ __attribute__((aligned(16))) char smem_[147456 + 10240];
  LAS char* smem = (LAS char*)smem_;

  const int b = blockIdx.x, x8 = b & 7, bi = b >> 3;
  const int rb = 8 * (x8 >> 1) + (bi & 7), cb = 4 * (x8 & 1) + (bi >> 3);
  const int tid = threadIdx.x, lane = tid & 63;
  const int w = __builtin_amdgcn_readfirstlane(tid >> 6);
  const int tg = w & 1, kq = w >> 1;
  const int l15 = lane & 15, q = lane >> 4;
  const int shift = (cb & 1) * 32;
  const int a0 = cb * 160 - shift;

  const u16* wsrc;
  {
    const int r = lane >> 2, cp_ = lane & 3, c = cp_ ^ (((r >> 2) & 1) << 1);
    wsrc = W + (size_t)(kq * KPW * 32 + r) * D_ + (a0 >> 1) + 8 * c;
  }
  auto issue = [&](int ch) {
    LAS char* s = smem + (ch % NSLOT) * SLOT + kq * CK * 6 * 1024;
#pragma unroll
    for (int j = 0; j < PPC; ++j) {
      const int p0 = 2 * j, p1 = 2 * j + 1;
      const int off0 = ((ch * CK + p0 / 6) * 32 + ((p0 % 6) / 3) * 16) * D_ + (p0 % 3) * 32;
      const int off1 = ((ch * CK + p1 / 6) * 32 + ((p1 % 6) / 3) * 16) * D_ + (p1 % 3) * 32;
      glds16(wsrc + (tg ? off1 : off0), s + (2 * j + tg) * 1024);
    }
  };
  u32x4 a[2][KPW];
  {
    const u32x4* ap = (const u32x4*)act + ((size_t)(rb * 4 + 2 * tg) * KS + kq * KPW) * 64 + lane;
#pragma unroll
    for (int s = 0; s < KPW; ++s) { a[0][s] = ap[s * 64]; a[1][s] = ap[(KS + s) * 64]; }
  }
  if (RES_RM) {
    for (int pc = w; pc < 10; pc += 8) {
      const int ci = pc * 64 + lane, row = ci / 10, c = ci - row * 10;
      glds16(res + (size_t)(rb * 64 + row) * D_ + cb * 80 + c * 8, smem + RESOFF + pc * 1024);
    }
  }
#pragma unroll
  for (int ch = 0; ch < (NSLOT > 1 ? NSLOT - 1 : 1); ++ch) issue(ch);

  int woff[5];
  {
    const int qq = (lane & 15) >> 2, p = lane & 3, r = 4 * q + qq;
#pragma unroll
    for (int ft = 0; ft < 5; ++ft) {
      const int col = shift + 32 * ft, seg = col >> 6, aa = (col >> 5) & 1;
      woff[ft] = kq * CK * 6 * 1024 + seg * 1024 + r * 64 + (((2 * aa + (p >> 1)) ^ ((q & 1) << 1)) * 16) + (p & 1) * 8;
    }
  }
  f32x4 acc[2][5];
#pragma unroll
  for (int i = 0; i < 2; ++i)
#pragma unroll
    for (int t = 0; t < 5; ++t) acc[i][t] = f32x4{0.f, 0.f, 0.f, 0.f};

#pragma unroll
  for (int ch = 0; ch < NCH; ++ch) {
    if (NSLOT == 1) wait_vm<0>();
    else { if (ch == NCH - 1) wait_vm<0>(); else wait_vm<PPC*(NSLOT > 1 ? NSLOT - 2 : 0)>(); }
    bar();
    if (NSLOT > 1 && ch + NSLOT - 1 < NCH) issue(ch + NSLOT - 1);
    const LAS char* s = smem + (ch % NSLOT) * SLOT;
#pragma unroll
    for (int sc = 0; sc < CK; ++sc) {
      u32x4 wf[5];
#pragma unroll
      for (int t = 0; t < 5; ++t) {
        u32x2 g0 = tr_read(s + woff[t] + sc * 6144);
        u32x2 g1 = tr_read(s + woff[t] + sc * 6144 + 3072);
        wf[t] = u32x4{g0.x, g0.y, g1.x, g1.y};
      }
#pragma unroll
      for (int i = 0; i < 2; ++i)
#pragma unroll
        for (int t = 0; t < 5; ++t) acc[i][t] = mfma16(wf[t], a[i][ch * CK + sc], acc[i][t]);
    }
  }
  u32x2 rv[3];
  if (!RES_RM) {
#pragma unroll
    for (int j = 0; j < 3; ++j) {
      const int pr = w + 8 * j;
      if (pr < 20) {
        const int tt = pr / 5, ft = pr - tt * 5, T = 5 * cb + ft;
        rv[j] = *(const u32x2*)(res + (((size_t)(rb * 4 + tt) * 20 + (T >> 1)) * 64 + lane) * 8 + (T & 1) * 4);
      }
    }
  }
  wait_lgkm0();
  bar();
#pragma unroll
  for (int i = 0; i < 2; ++i)
#pragma unroll
    for (int t = 0; t < 5; ++t)
      *(LAS f32x4*)(smem + (kq * 64 + (2 * tg + i) * 16 + l15) * RSTRIDE + (16 * t + 4 * q) * 4) = acc[i][t];
  wait_lgkm0();
  bar();
#pragma unroll
  for (int j = 0; j < 3; ++j) {
    const int pr = w + 8 * j;
    if (pr < 20) {
      const int tt = pr / 5, ft = pr - tt * 5, T = 5 * cb + ft;
      const int tok = tt * 16 + l15;
      f32x4 v = *(const LAS f32x4*)(smem + tok * RSTRIDE + (16 * ft + 4 * q) * 4);
#pragma unroll
      for (int k = 1; k < 4; ++k) v += *(const LAS f32x4*)(smem + (k * 64 + tok) * RSTRIDE + (16 * ft + 4 * q) * 4);
      u32x2 r2;
      if (RES_RM) r2 = lds_b64(smem + RESOFF + tok * 160 + (16 * ft + 4 * q) * 2);
      else r2 = rv[j];
      const unsigned r0 = r2.x, r1 = r2.y;
      u32x2 o = u32x2{pack2(bf_lo(r0) + v[0], bf_hi(r0) + v[1]), pack2(bf_lo(r1) + v[2], bf_hi(r1) + v[3])};
      if (OUT_RM) *(u32x2*)(out + (size_t)(rb * 64 + tok) * D_ + cb * 80 + 16 * ft + 4 * q) = o;
      else *(u32x2*)(out + (((size_t)(rb * 4 + tt) * 20 + (T >> 1)) * 64 + lane) * 8 + (T & 1) * 4) = o;
    }
  }
}

__global__ __launch_bounds__(512, 2) void attn_k(const u16* __restrict__ Qt, const u16* __restrict__ Kt,
                                                 const u16* __restrict__ Vlo, const u16* __restrict__ Vhi,
                                                 u16* __restrict__ o_t) {
  __shared__ __attribute__((aligned(16))) char smem_[40960];
  LAS char* smem = (LAS char*)smem_;
  const int b = blockIdx.x, x8 = b & 7, bi = b >> 3;
  const int h = 4 * x8 + (bi & 3), qb = bi >> 2;
  const int tid = threadIdx.x, lane = tid & 63;
  const int w = __builtin_amdgcn_readfirstlane(tid >> 6);
  const int l15 = lane & 15, q = lane >> 4;
  {
    const char* kb = (const char*)Kt + ((size_t)h * 128 + 16 * qb - 16) * 640;
    const char* vl = (const char*)Vlo + ((size_t)h * 64 + 8 * qb - 8) * 1024;
    const char* vh = (const char*)Vhi + ((size_t)h * 64 + 8 * qb - 8) * 256;
#pragma unroll
    for (int j = 0; j < 5; ++j) {
      const int pc = w + 8 * j;
      const char* src; bool first_half;
      if (pc < 20) { src = kb + pc * 1024; first_half = pc < 10; }
      else if (pc < 36) { src = vl + (pc - 20) * 1024; first_half = pc < 28; }
      else { src = vh + (pc - 36) * 1024; first_half = pc < 38; }
      if (qb == 0 && first_half) *(LAS u32x4*)(smem + pc * 1024 + lane * 16) = u32x4{0, 0, 0, 0};
      else glds16(src + lane * 16, smem + pc * 1024);
    }
  }
  const int TT0 = 16 * qb + 2 * w;
  u32x4 qf[2];
#pragma unroll
  for (int i = 0; i < 2; ++i) {
    const u16* qp = Qt + ((size_t)h * 128 + TT0 + i) * 320;
    u32x2 lo = *(const u32x2*)(qp + q * 64 + l15 * 4);
    u32x2 hi = *(const u32x2*)(qp + 256 + l15 * 4);
    qf[i] = u32x4{lo.x, lo.y, q == 0 ? hi.x : 0u, q == 0 ? hi.y : 0u};
  }
  wait_vm<0>();
  wait_lgkm0();
  bar();
  const LAS char* kimg = smem + 2 * w * 640 + l15 * 8;
  f32x4 sa[17], sb[17];
  const f32x4 zero4 = f32x4{0.f, 0.f, 0.f, 0.f};
#pragma unroll
  for (int j = 0; j < 18; ++j) {
    u32x2 lo = lds_b64(kimg + j * 640 + q * 128);
    u32x2 hi = lds_b64(kimg + j * 640 + 512);
    u32x4 kf = u32x4{lo.x, lo.y, hi.x, hi.y};
    if (j < 17) sa[j] = mfma16(kf, qf[0], zero4);
    if (j > 0) sb[j - 1] = mfma16(kf, qf[1], zero4);
  }
  const float NEG = -1e30f;
#pragma unroll
  for (int r = 0; r < 4; ++r) {
    const bool gt = (4 * q + r) > l15;
    sa[0][r] = gt ? sa[0][r] : NEG; sb[0][r] = gt ? sb[0][r] : NEG;
    sa[16][r] = gt ? NEG : sa[16][r]; sb[16][r] = gt ? NEG : sb[16][r];
  }
  if (qb == 0) {
#pragma unroll
    for (int j = 0; j < 16; ++j) {
      if (2 * w + j < 16) sa[j] = f32x4{NEG, NEG, NEG, NEG};
      if (2 * w + j + 1 < 16) sb[j] = f32x4{NEG, NEG, NEG, NEG};
    }
  }
  float ma = NEG, mbx = NEG;
#pragma unroll
  for (int j = 0; j < 17; ++j) {
    ma = fmaxf(ma, fmaxf(fmaxf(sa[j][0], sa[j][1]), fmaxf(sa[j][2], sa[j][3])));
    mbx = fmaxf(mbx, fmaxf(fmaxf(sb[j][0], sb[j][1]), fmaxf(sb[j][2], sb[j][3])));
  }
  ma = fmaxf(ma, __shfl_xor(ma, 16)); ma = fmaxf(ma, __shfl_xor(ma, 32));
  mbx = fmaxf(mbx, __shfl_xor(mbx, 16)); mbx = fmaxf(mbx, __shfl_xor(mbx, 32));
  const float C = 0.22360679775f * 1.44269504089f;
  const float mca = ma * C, mcb = mbx * C;
  unsigned pa[18][2], pb[18][2];
#pragma unroll
  for (int j = 0; j < 17; ++j) {
    pa[j][0] = pack2(__builtin_amdgcn_exp2f(sa[j][0] * C - mca), __builtin_amdgcn_exp2f(sa[j][1] * C - mca));
    pa[j][1] = pack2(__builtin_amdgcn_exp2f(sa[j][2] * C - mca), __builtin_amdgcn_exp2f(sa[j][3] * C - mca));
    pb[j + 1][0] = pack2(__builtin_amdgcn_exp2f(sb[j][0] * C - mcb), __builtin_amdgcn_exp2f(sb[j][1] * C - mcb));
    pb[j + 1][1] = pack2(__builtin_amdgcn_exp2f(sb[j][2] * C - mcb), __builtin_amdgcn_exp2f(sb[j][3] * C - mcb));
  }
  pa[17][0] = pa[17][1] = 0u; pb[0][0] = pb[0][1] = 0u;
  f32x4 oal = zero4, oah = zero4, obl = zero4, obh = zero4;
  const LAS char* vlo = smem + 20480 + w * 1024 + lane * 16;
  const LAS char* vhi = smem + 36864 + w * 256 + q * 64 + (l15 < 3 ? l15 : 3) * 16;
  const bool one = (l15 == 4);
#pragma unroll
  for (int u = 0; u < 9; ++u) {
    u32x4 vl = lds_b128(vlo + u * 1024);
    u32x4 vh = lds_b128(vhi + u * 256);
    vh.x = one ? 0x3f803f80u : vh.x; vh.y = one ? 0x3f803f80u : vh.y;
    vh.z = one ? 0x3f803f80u : vh.z; vh.w = one ? 0x3f803f80u : vh.w;
    u32x4 fa = u32x4{pa[2 * u][0], pa[2 * u][1], pa[2 * u + 1][0], pa[2 * u + 1][1]};
    u32x4 fb = u32x4{pb[2 * u][0], pb[2 * u][1], pb[2 * u + 1][0], pb[2 * u + 1][1]};
    oal = mfma16(vl, fa, oal); oah = mfma16(vh, fa, oah);
    obl = mfma16(vl, fb, obl); obh = mfma16(vh, fb, obh);
  }
  const float ia = __builtin_amdgcn_rcpf(__shfl(oah[0], 16 + l15));
  const float ib = __builtin_amdgcn_rcpf(__shfl(obh[0], 16 + l15));
  {
    const int g = h * 5 + q, g4 = h * 5 + 4;
    const int offl = (((g >> 3) * 64) + (g & 3) * 16 + l15) * 8 + ((g >> 2) & 1) * 4;
    const int offh = (((g4 >> 3) * 64) + (g4 & 3) * 16 + l15) * 8 + ((g4 >> 2) & 1) * 4;
    u16* oa = o_t + (size_t)TT0 * 20 * 512;
    u16* ob = oa + 20 * 512;
    *(u32x2*)(oa + offl) = u32x2{pack2(oal[0] * ia, oal[1] * ia), pack2(oal[2] * ia, oal[3] * ia)};
    *(u32x2*)(ob + offl) = u32x2{pack2(obl[0] * ib, obl[1] * ib), pack2(obl[2] * ib, obl[3] * ib)};
    if (q == 0) {
      *(u32x2*)(oa + offh) = u32x2{pack2(oah[0] * ia, oah[1] * ia), pack2(oah[2] * ia, oah[3] * ia)};
      *(u32x2*)(ob + offh) = u32x2{pack2(obh[0] * ib, obh[1] * ib), pack2(obh[2] * ib, obh[3] * ib)};
    }
  }
}

extern "C" void kernel_launch(void* const* d_in, const int* in_sizes, int n_in, void* d_out, int out_size,
                              void* d_ws, size_t ws_size, hipStream_t stream) {
  const u16* x = (const u16*)d_in[0];
  const u16* wqkv = (const u16*)d_in[1];
  const u16* wo = (const u16*)d_in[2];
  const u16* wg = (const u16*)d_in[4];
  const u16* wu = (const u16*)d_in[5];
  const u16* wd = (const u16*)d_in[6];
  u16* ws = (u16*)d_ws;
  const size_t SD = (size_t)S_ * D_;
  u16* Qt = ws; u16* Kt = Qt + SD; u16* Vlo = Kt + SD; u16* Vhi = Vlo + (size_t)S_ * 512;
  u16* o_t = Vhi + (size_t)S_ * 128; u16* x1 = o_t + SD; u16* x2 = x1 + SD; u16* x3 = x2 + SD;
  u16* h_t = x3 + SD;
  u16* out = (u16*)d_out;
  gemm_ln<0><<<256, 512, 0, stream>>>(x, wqkv, nullptr, Qt, Kt, Vlo, Vhi);
  attn_k<<<256, 512, 0, stream>>>(Qt, Kt, Vlo, Vhi, o_t);
  gemm_res<20, 1, 0><<<256, 512, 0, stream>>>(o_t, wo, x, x1);
  gemm_ln<2><<<256, 512, 0, stream>>>(x1, wg, wu, h_t, nullptr, nullptr, nullptr);
  gemm_res<48, 0, 0><<<256, 512, 0, stream>>>(h_t, wd, x1, x2);
  gemm_ln<1><<<256, 512, 0, stream>>>(x2, wqkv, nullptr, Qt, Kt, Vlo, Vhi);
  attn_k<<<256, 512, 0, stream>>>(Qt, Kt, Vlo, Vhi, o_t);
  gemm_res<20, 0, 0><<<256, 512, 0, stream>>>(o_t, wo, x2, x3);
  gemm_ln<2><<<256, 512, 0, stream>>>(x3, wg, wu, h_t, nullptr, nullptr, nullptr);
  gemm_res<48, 0, 1><<<256, 512, 0, stream>>>(h_t, wd, x3, out);
}
```

```cpp
#include <hip/hip_runtime.h>
#include <hip/hip_bf16.h>
#include <stdint.h>
#include <type_traits>

typedef __bf16 bf16x8 __attribute__((ext_vector_type(8)));
typedef __bf16 bf16x2 __attribute__((ext_vector_type(2)));
typedef short s16x4 __attribute__((ext_vector_type(4)));
typedef float f32x4 __attribute__((ext_vector_type(4)));
typedef float f32x2 __attribute__((ext_vector_type(2)));
typedef unsigned u32x4 __attribute__((ext_vector_type(4)));
typedef unsigned u32x2 __attribute__((ext_vector_type(2)));
typedef unsigned short u16;
typedef unsigned long long u64;
#define LAS __attribute__((address_space(3)))
#define DEV __device__ __forceinline__

constexpr int S_ = 2048, D_ = 640, F_ = 1536;

#define STAMP(i) do {} while (0)

DEV f32x4 mfma16(u32x4 a, u32x4 b, f32x4 c) {
  return __builtin_amdgcn_mfma_f32_16x16x32_bf16(__builtin_bit_cast(bf16x8, a), __builtin_bit_cast(bf16x8, b), c, 0, 0, 0);
}
DEV unsigned pack2(float lo, float hi) {
  f32x2 p = {lo, hi};
  bf16x2 q = __builtin_convertvector(p, bf16x2);
  return __builtin_bit_cast(unsigned, q);
}
DEV float bf_lo(unsigned u) { return __builtin_bit_cast(float, u << 16); }
DEV float bf_hi(unsigned u) { return __builtin_bit_cast(float, u & 0xffff0000u); }
DEV float rbf(float x) { return bf_lo(pack2(x, 0.f)); }
DEV u32x2 tr_read(const LAS char* p) {
#if defined(__HIP_DEVICE_COMPILE__)
  s16x4 v = __builtin_amdgcn_ds_read_tr16_b64_v4i16((LAS s16x4*)p);
  return __builtin_bit_cast(u32x2, v);
#else
  return u32x2{0, 0};
#endif
}
DEV float xsum(float v) {
#if defined(__HIP_DEVICE_COMPILE__)
  unsigned u = __builtin_bit_cast(unsigned, v);
  auto r = __builtin_amdgcn_permlane32_swap(u, u, false, false);
  unsigned r0 = r[0], r1 = r[1];
  float s = __builtin_bit_cast(float, r0) + __builtin_bit_cast(float, r1);
  unsigned u2 = __builtin_bit_cast(unsigned, s);
  auto t = __builtin_amdgcn_permlane16_swap(u2, u2, false, false);
  unsigned t0 = t[0], t1 = t[1];
  return __builtin_bit_cast(float, t0) + __builtin_bit_cast(float, t1);
#else
  return v;
#endif
}
DEV float xmax(float v) {
#if defined(__HIP_DEVICE_COMPILE__)
  unsigned u = __builtin_bit_cast(unsigned, v);
  auto r = __builtin_amdgcn_permlane32_swap(u, u, false, false);
  unsigned r0 = r[0], r1 = r[1];
  float s = fmaxf(__builtin_bit_cast(float, r0), __builtin_bit_cast(float, r1));
  unsigned u2 = __builtin_bit_cast(unsigned, s);
  auto t = __builtin_amdgcn_permlane16_swap(u2, u2, false, false);
  unsigned t0 = t[0], t1 = t[1];
  return fmaxf(__builtin_bit_cast(float, t0), __builtin_bit_cast(float, t1));
#else
  return v;
#endif
}
template <int OFF, int SC1> DEV void glds16(const void* g, LAS char* l) {
  unsigned keep;
  const unsigned m = (unsigned)(uintptr_t)l - OFF;
  if (SC1)
    asm volatile("s_mov_b32 %0, m0\n\ts_mov_b32 m0, %2\n\ts_nop 0\n\tglobal_load_lds_dwordx4 %1, off offset:%3 sc1\n\ts_mov_b32 m0, %0"
                 : "=&s"(keep) : "v"(g), "s"(m), "n"(OFF) : "memory");
  else
    asm volatile("s_mov_b32 %0, m0\n\ts_mov_b32 m0, %2\n\ts_nop 0\n\tglobal_load_lds_dwordx4 %1, off offset:%3\n\ts_mov_b32 m0, %0"
                 : "=&s"(keep) : "v"(g), "s"(m), "n"(OFF) : "memory");
}
template <int OFF> DEV void gld16_sc1(u32x4& d, const void* g) {
  asm volatile("global_load_dwordx4 %0, %1, off offset:%2 sc1" : "=v"(d) : "v"(g), "n"(OFF) : "memory");
}
template <int OFF> DEV void gld8_sc1(u32x2& d, const void* g) {
  asm volatile("global_load_dwordx2 %0, %1, off offset:%2 sc1" : "=v"(d) : "v"(g), "n"(OFF) : "memory");
}
DEV void gst8_sc1(void* p, u32x2 v) {
  asm volatile("global_store_dwordx2 %0, %1, off sc1" ::"v"(p), "v"(v) : "memory");
}
DEV void gst16_sc1(void* p, u32x4 v) {
  asm volatile("global_store_dwordx4 %0, %1, off sc1\n\ts_nop 1" ::"v"(p), "v"(v) : "memory");
}
template <int OFF> DEV void gld16f_sc1(f32x4& d, const void* g) {
  asm volatile("global_load_dwordx4 %0, %1, off offset:%2 sc1" : "=v"(d) : "v"(g), "n"(OFF) : "memory");
}
template <int OFF> DEV void gld8f_sc1(f32x2& d, const void* g) {
  asm volatile("global_load_dwordx2 %0, %1, off offset:%2 sc1" : "=v"(d) : "v"(g), "n"(OFF) : "memory");
}
template <int N> DEV void wait_vm() { asm volatile("s_waitcnt vmcnt(%0)" ::"n"(N) : "memory"); }
DEV void bar() { asm volatile("s_waitcnt lgkmcnt(0)" ::: "memory"); __builtin_amdgcn_s_barrier(); asm volatile("" ::: "memory"); }
DEV u32x4 lds_b128(const LAS char* p) { return *(const LAS u32x4*)p; }
DEV u32x2 lds_b64(const LAS char* p) { return *(const LAS u32x2*)p; }
template <class T> DEV void pin(T& x) { asm volatile("" : "+v"(x)); }
DEV f32x2 lo2(f32x4 v) { return f32x2{v[0], v[1]}; }
DEV f32x2 hi2(f32x4 v) { return f32x2{v[2], v[3]}; }
DEV f32x2 fma2(f32x2 a, f32x2 b, f32x2 c) { return __builtin_elementwise_fma(a, b, c); }
DEV unsigned pack2v(f32x2 p) { bf16x2 q = __builtin_convertvector(p, bf16x2); return __builtin_bit_cast(unsigned, q); }
DEV void sched_fence() { __builtin_amdgcn_sched_barrier(0); }

constexpr int SY_EPOCH = 0, SY_START = 32, SY_CS = 320, SY_FQ = 512, SY_FA = 1024, SY_FW = 1536, SY_FG = 2048, SY_FD = 2560;
constexpr int AUX_CSQ = 32768, AUX_CSG = AUX_CSQ + 1920 * 4, AUX_ST = 65536, AUX_ST_BYTES = 2048 * 8 * 8, WS_ACT = AUX_ST + 4 * AUX_ST_BYTES;
constexpr unsigned SPIN_CAP = 1u << 16;

struct Ctx {
  int tid, lane, w, l15, q, x8, bi;
  LAS char* smem;
  u64* sync;
  u64 tag;
};

DEV Ctx fresh(const Ctx& o) {
  Ctx c = o;
  int t = threadIdx.x; asm volatile("" : "+v"(t));
  int b = blockIdx.x; asm volatile("" : "+s"(b));
  c.tid = t; c.lane = t & 63; c.w = __builtin_amdgcn_readfirstlane(t >> 6);
  c.l15 = c.lane & 15; c.q = c.lane >> 4; c.x8 = b & 7; c.bi = b >> 3;
  return c;
}
DEV void poll_flags(const Ctx& c, const u64* p, bool valid) {
  if (c.w == 0) {
    unsigned spins = 0;
    while (true) {
      u64 v = __hip_atomic_load(p, __ATOMIC_RELAXED, __HIP_MEMORY_SCOPE_AGENT);
      if (__all(!valid || v == c.tag)) break;
      if (++spins > SPIN_CAP) break;
    }
  }
  bar();
}
template <int I, int N, class F> DEV void static_for(F&& f) {
  if constexpr (I < N) { f(std::integral_constant<int, I>{}); static_for<I + 1, N>(f); }
}
DEV void set_flag(const Ctx& c, u64* p) {
  if (c.tid == 0) __hip_atomic_store(p, c.tag, __ATOMIC_RELAXED, __HIP_MEMORY_SCOPE_AGENT);
}

template <int MODE> struct FCfg {
  static constexpr int NCP = (MODE == 2) ? 3 : 2;
  static constexpr int NS = (MODE == 2) ? 4 : 5;
  static constexpr int WSZ = 8 * NCP * 1024;
  static constexpr int WB0 = NS * 16384;
  static constexpr int WROT = 1;
  static constexpr int NFT = (MODE == 2) ? 6 : 4;
  static constexpr int NF = (MODE == 2) ? 192 : 128;
  static constexpr int LDW = (MODE == 2) ? F_ : 3 * D_;
  static constexpr int WCPB = (MODE == 2) ? 64 : 128;
  static constexpr int STOFF = (MODE == 2) ? 147456 : 0;
};

template <int MODE> DEV const u16* f_wsrc(const Ctx& c, const u16* W0, const u16* W1, int cb) {
  const int r = c.lane >> 3, cp_ = c.lane & 7, cc = cp_ ^ (((r >> 1) & 3) << 1);
  int row = 8 * c.w + r;
  if (MODE == 0) row = 32 * (c.w >> 2) + 8 * (2 * (c.w & 1) + (r >> 2)) + 4 * ((c.w & 3) >> 1) + (r & 3);
  if (MODE == 2) return ((cc < 4) ? W0 : W1) + (size_t)row * FCfg<MODE>::LDW + 96 * cb + 8 * (cc & 3);
  return W0 + (size_t)row * FCfg<MODE>::LDW + 128 * cb + 8 * cc;
}
template <int MODE, int PIECE> DEV void f_issueW1(const Ctx& c, const u16* wsrc, int wslot) {
  using C = FCfg<MODE>;
  LAS char* s = c.smem + C::WB0 + wslot * C::WSZ + (c.w * C::NCP + PIECE) * 1024;
  glds16<PIECE * C::WCPB, 0>(wsrc, s);
}
template <int MODE> DEV void f_issueW(const Ctx& c, const u16*& wsrc, int wslot) {
  using C = FCfg<MODE>;
  f_issueW1<MODE, 0>(c, wsrc, wslot);
  f_issueW1<MODE, 1>(c, wsrc, wslot);
  if (C::NCP == 3) f_issueW1<MODE, 2>(c, wsrc, wslot);
  wsrc += 64 * C::LDW;
}
template <int MODE, int PIECE> DEV void f_issueA1(const Ctx& c, const u16* a0, const u16* a1, int aslot) {
  LAS char* s = c.smem + aslot * 16384 + c.w * 2048 + PIECE * 1024;
  if (MODE == 0) glds16<0, 0>(PIECE ? a1 : a0, s);
  else glds16<PIECE * 1024, 1>(a0, s);
}
template <int MODE> DEV void f_issueA(const Ctx& c, const u16*& a0, const u16*& a1, int aslot) {
  f_issueA1<MODE, 0>(c, a0, a1, aslot);
  f_issueA1<MODE, 1>(c, a0, a1, aslot);
  if (MODE == 0) { a0 += 64; a1 += 64; } else a0 += 1024;
}

template <int MODE, int VARIANT>
DEV void f_main(const Ctx& c, const u16* wsrc, const u16* a0, const u16* a1, int rb, int cb, u16* o0, const char* stp, char* csg,
                const u64* pollp, bool pollv) {
  using C = FCfg<MODE>;
  constexpr int NCP = C::NCP, NS = C::NS, NFT = C::NFT, PPC = 2 + NCP;
  const int lane = c.lane, l15 = c.l15, q = c.q, w = c.w;
  const int tg = w & 3, fh = w >> 2;
  LAS char* smem = c.smem;
  int woff[NFT], woffc = 0;
  {
    const int qq = (lane & 15) >> 2, p = lane & 3, r = 4 * (q & 1) + qq, swz = ((r >> 1) & 3) << 1;
    const int base = C::WB0 + (q >> 1) * NCP * 1024 + r * 128 + (p >> 1) * 16 + (p & 1) * 8;
    if (MODE == 2) {
#pragma unroll
      for (int t = 0; t < 3; ++t) {
        const int T = 3 * fh + t;
        woff[t] = base + (((2 * (T & 1)) ^ swz) * 16) + (T >> 1) * 1024;
        woff[3 + t] = base + (((4 + 2 * (T & 1)) ^ swz) * 16) + (T >> 1) * 1024;
      }
    } else {
#pragma unroll
      for (int t = 0; t < 4; ++t) woff[t] = base + (((2 * t) ^ swz) * 16) + fh * 1024;
      woffc = base + (((2 * tg) ^ swz) * 16) + fh * 1024;
    }
  }
  int aoff[2][2];
#pragma unroll
  for (int i = 0; i < 2; ++i)
#pragma unroll
    for (int ks = 0; ks < 2; ++ks) {
      const int T = 2 * tg + (i ^ fh);
      if (MODE == 0) aoff[i][ks] = T * 2048 + l15 * 128 + (((4 * ks + q) ^ ((l15 >> 1) & 7)) * 16);
      else aoff[i][ks] = T * 2048 + ks * 1024 + lane * 16;
    }
  f32x4 acc[2][NFT];
#pragma unroll
  for (int i = 0; i < 2; ++i)
#pragma unroll
    for (int t = 0; t < NFT; ++t) acc[i][t] = f32x4{0.f, 0.f, 0.f, 0.f};
  f32x4 a_s = f32x4{0.f, 0.f, 0.f, 0.f}, a_q = a_s, a_c = a_s;
  const u32x4 ones = u32x4{0x3f803f80u, 0x3f803f80u, 0x3f803f80u, 0x3f803f80u};

  auto readf = [&](int aslot_off, int wslot_off, int ks, u32x4 (&a)[2], u32x4 (&wf)[NFT], u32x4& wc) {
    const LAS char* sa = smem + aslot_off;
    const LAS char* sw = smem + wslot_off;
#pragma unroll
    for (int i = 0; i < 2; ++i) a[i] = lds_b128(sa + aoff[i][ks]);
#pragma unroll
    for (int t = 0; t < NFT; ++t) {
      u32x2 g0 = tr_read(sw + woff[t] + (4 * ks) * NCP * 1024);
      u32x2 g1 = tr_read(sw + woff[t] + (4 * ks + 2) * NCP * 1024);
      wf[t] = u32x4{g0.x, g0.y, g1.x, g1.y};
    }
    if (MODE == 0) {
      u32x2 g0 = tr_read(sw + woffc + (4 * ks) * NCP * 1024);
      u32x2 g1 = tr_read(sw + woffc + (4 * ks + 2) * NCP * 1024);
      wc = u32x4{g0.x, g0.y, g1.x, g1.y};
    }
  };
  auto mm = [&](const u32x4 (&a)[2], const u32x4 (&wf)[NFT], int t) {
#pragma unroll
    for (int i = 0; i < 2; ++i) acc[i][t] = mfma16(wf[t], a[i], acc[i][t]);
  };
  auto extra = [&](const u32x4 (&a)[2], const u32x4& wc) {
    if (MODE == 0) { a_s = mfma16(ones, a[0], a_s); a_q = mfma16(a[0], a[0], a_q); a_c = mfma16(wc, ones, a_c); pin(a_s); pin(a_q); pin(a_c); }
  };

  u32x4 aA[2], wA[NFT], aB[2], wB[NFT], wcA, wcB;
  constexpr int KEX = (MODE == 0) ? 0 : 4 + NFT;
  f32x2 sp[2][2];
  f32x4 cg[NFT];
  u16* dst[4];
  if (MODE == 2) {
    dst[0] = o0 + (((size_t)(rb * 8 + 2 * tg + fh) * 48 + 3 * cb) * 64 + lane) * 8 + (fh ? 516 : 0);
    dst[1] = o0 + (((size_t)(rb * 8 + 2 * tg + (1 ^ fh)) * 48 + 3 * cb) * 64 + lane) * 8 + (fh ? 516 : 0);
    pin(dst[0]); pin(dst[1]);
  } else {
#pragma unroll
    for (int t = 0; t < 4; ++t) {
      const int g = 32 * cb + 16 * fh + 4 * t + q;
      const int head = (int)(((unsigned)g * 52429u) >> 18), dg = g - head * 5;
      dst[t] = o0 + (((size_t)head * 128 + rb * 8 + 2 * tg) * 5 + dg) * 64 + ((l15 ^ (((dg >> 1) & 1) << 3)) * 4);
      pin(dst[t]);
    }
  }
  const char* stq = stp + (((rb * 8 + 2 * tg) * 8 + 2 * q) * 16 + l15) * 8;
  const char* csq = csg + ((MODE == 2) ? (cb * 192 + fh * 48 + 4 * q) * 4 : (cb * 128 + fh * 64 + 4 * q) * 4);
  if (MODE != 0) { pin(stq); pin(csq); }
#pragma unroll
  for (int t = 0; t < NFT; ++t) pin(woff[t]);
  pin(aoff[0][0]); pin(aoff[1][0]);
  if (VARIANT == 1) {
    pin(a0);
    poll_flags(c, pollp, pollv);
    f_issueA<MODE>(c, a0, a1, 0); f_issueA<MODE>(c, a0, a1, 1);
  }
  if (VARIANT == 0) wait_vm<PPC>(); else wait_vm<2>();
  bar();
  readf(0, C::WROT * C::WSZ, 0, aA, wA, wcA);
#pragma unroll
  for (int ch = 2; ch < NS; ++ch) {
    if (VARIANT == 0) f_issueW<MODE>(c, wsrc, (ch + C::WROT) % NS);
    f_issueA<MODE>(c, a0, a1, ch);
  }
  auto iter = [&](auto CH_) {
    constexpr int ch = decltype(CH_)::value;
    constexpr int as_c = ch % NS, ws_c = (ch + C::WROT) % NS, as_n = (ch + 1) % NS, ws_n = (ch + 1 + C::WROT) % NS;
    constexpr bool pendA = (ch >= 1) && (ch - 1 + NS < 10);
    constexpr bool pendW = (ch + NS < 10);
    constexpr int as_p = (ch + NS - 1) % NS;
    readf(as_c * 16384, ws_c * C::WSZ, 1, aB, wB, wcB);
    sched_fence();
    mm(aA, wA, 0); mm(aA, wA, 1);
    sched_fence();
    if (pendA) f_issueA1<MODE, 0>(c, a0, a1, as_p);
    sched_fence();
    mm(aA, wA, 2); mm(aA, wA, 3);
    sched_fence();
    if (pendA) { f_issueA1<MODE, 1>(c, a0, a1, as_p); if (MODE == 0) { a0 += 64; a1 += 64; } else a0 += 1024; }
    sched_fence();
    if (NFT == 6) { mm(aA, wA, NFT - 2); mm(aA, wA, NFT - 1); }
    extra(aA, wcA);
    constexpr int last = (ch + NS - 1) < 9 ? (ch + NS - 1) : 9;
    constexpr int n = ((VARIANT == 0 || ch + 1 >= NS) ? (last - (ch + 1)) * PPC : (NS - 1 - (ch + 1)) * 2 + (last - (NS - 1)) * PPC) + (ch == 8 ? KEX : 0);
    wait_vm<n>();
    bar();
    readf(as_n * 16384, ws_n * C::WSZ, 0, aA, wA, wcA);
    sched_fence();
    if (MODE != 0 && ch == 7) {
      gld8f_sc1<0>(sp[0][0], stq); gld8f_sc1<128>(sp[0][1], stq); gld8f_sc1<1024>(sp[1][0], stq); gld8f_sc1<1152>(sp[1][1], stq);
      if (MODE == 2) {
        gld16f_sc1<0>(cg[0], csq); gld16f_sc1<64>(cg[1], csq); gld16f_sc1<128>(cg[2], csq);
        gld16f_sc1<384>(cg[NFT - 3], csq); gld16f_sc1<448>(cg[NFT - 2], csq); gld16f_sc1<512>(cg[NFT - 1], csq);
      } else {
        gld16f_sc1<0>(cg[0], csq); gld16f_sc1<64>(cg[1], csq); gld16f_sc1<128>(cg[2], csq); gld16f_sc1<192>(cg[3], csq);
      }
    }
    if (pendW) f_issueW1<MODE, 0>(c, wsrc, ws_c);
    sched_fence();
    mm(aB, wB, 0); mm(aB, wB, 1);
    sched_fence();
    if (pendW) f_issueW1<MODE, 1>(c, wsrc, ws_c);
    sched_fence();
    mm(aB, wB, 2); mm(aB, wB, 3);
    sched_fence();
    if (pendW) { if (NCP == 3) f_issueW1<MODE, 2>(c, wsrc, ws_c); wsrc += 64 * C::LDW; }
    sched_fence();
    if (NFT == 6) { mm(aB, wB, NFT - 2); mm(aB, wB, NFT - 1); }
    extra(aB, wcB);
  };
  iter(std::integral_constant<int, 0>{}); iter(std::integral_constant<int, 1>{}); iter(std::integral_constant<int, 2>{});
  iter(std::integral_constant<int, 3>{}); iter(std::integral_constant<int, 4>{}); iter(std::integral_constant<int, 5>{});
  iter(std::integral_constant<int, 6>{}); iter(std::integral_constant<int, 7>{}); iter(std::integral_constant<int, 8>{});
  readf((9 % NS) * 16384, ((9 + C::WROT) % NS) * C::WSZ, 1, aB, wB, wcB);
  sched_fence();
#pragma unroll
  for (int t = 0; t < NFT; ++t) mm(aA, wA, t);
  extra(aA, wcA);
#pragma unroll
  for (int t = 0; t < NFT; ++t) mm(aB, wB, t);
  extra(aB, wcB);
  f32x2 ms[2];
  auto finish = [&](float sx, float sxx) {
    const float mean = sx * (1.f / D_);
    const float var = fmaxf(sxx * (1.f / D_) - mean * mean, 0.f);
    const float sd = rbf(sqrtf(rbf(rbf(var) + 1e-5f)));
    return f32x2{rbf(mean), 1.f / sd};
  };
  if (MODE == 0) {
    LAS float* st = (LAS float*)(smem + C::STOFF);
    LAS float* csp = st + 256;
    if ((l15 >> 2) == q) {
      const int r = l15 & 3;
      const float sxx = (r == 0) ? a_q[0] : (r == 1) ? a_q[1] : (r == 2) ? a_q[2] : a_q[3];
      *(LAS f32x2*)(st + 2 * ((2 * tg + fh) * 16 + l15)) = finish(a_s[0], sxx);
    }
    if (l15 == 0) {
      *(LAS f32x4*)(csp + (fh * 4 + tg) * 16 + 4 * q) = a_c;
      if (rb == 0) gst16_sc1(csg + (cb * 128 + (fh * 4 + tg) * 16 + 4 * q) * 4, __builtin_bit_cast(u32x4, a_c));
    }
    bar();
#pragma unroll
    for (int t = 0; t < NFT; ++t) cg[t] = *(const LAS f32x4*)(csp + (fh * 4 + t) * 16 + 4 * q);
#pragma unroll
    for (int i = 0; i < 2; ++i) ms[i] = *(const LAS f32x2*)(st + 2 * ((2 * tg + (i ^ fh)) * 16 + l15));
  } else {
    if (NFT == 6)
      asm volatile("s_waitcnt vmcnt(0)" : "+v"(sp[0][0]), "+v"(sp[0][1]), "+v"(sp[1][0]), "+v"(sp[1][1]), "+v"(cg[0]), "+v"(cg[1]),
                   "+v"(cg[2]), "+v"(cg[3]), "+v"(cg[NFT - 2]), "+v"(cg[NFT - 1]) : : "memory");
    else
      asm volatile("s_waitcnt vmcnt(0)" : "+v"(sp[0][0]), "+v"(sp[0][1]), "+v"(sp[1][0]), "+v"(sp[1][1]), "+v"(cg[0]), "+v"(cg[1]),
                   "+v"(cg[2]), "+v"(cg[3]) : : "memory");
    const f32x2 m0 = finish(xsum(sp[0][0].x + sp[0][1].x), xsum(sp[0][0].y + sp[0][1].y));
    const f32x2 m1 = finish(xsum(sp[1][0].x + sp[1][1].x), xsum(sp[1][0].y + sp[1][1].y));
    ms[0] = fh ? m1 : m0; ms[1] = fh ? m0 : m1;
    bar();
  }
  if (MODE == 2) {
#pragma unroll
    for (int i = 0; i < 2; ++i) {
      const float rs = ms[i].y, mrs = ms[i].x * rs;
      const f32x2 rs2 = f32x2{rs, rs}, nm2 = f32x2{-mrs, -mrs};
      const f32x2 rg2 = rs2 * (-1.44269504089f), ng2 = nm2 * (-1.44269504089f);
      const f32x2 one2 = f32x2{1.f, 1.f};
      u32x2 o[3];
#pragma unroll
      for (int t = 0; t < 3; ++t) {
        unsigned pk[2];
#pragma unroll
        for (int hh = 0; hh < 2; ++hh) {
          const f32x2 ag = hh ? hi2(acc[i][t]) : lo2(acc[i][t]), cgg = hh ? hi2(cg[t]) : lo2(cg[t]);
          const f32x2 au = hh ? hi2(acc[i][NFT - 3 + t]) : lo2(acc[i][NFT - 3 + t]), cgu = hh ? hi2(cg[NFT - 3 + t]) : lo2(cg[NFT - 3 + t]);
          const f32x2 g = fma2(cgg, ng2, ag * rg2);
          const f32x2 u = fma2(cgu, nm2, au * rs2);
          const f32x2 d = f32x2{__builtin_amdgcn_exp2f(g.x), __builtin_amdgcn_exp2f(g.y)} + one2;
          pk[hh] = pack2v(u * f32x2{__builtin_amdgcn_rcpf(d.x), __builtin_amdgcn_rcpf(d.y)});
        }
        o[t] = u32x2{pk[0], pk[1]};
      }
      if (fh == 0) {
        gst16_sc1(dst[i], u32x4{o[0].x, o[0].y, o[1].x, o[1].y});
        gst8_sc1(dst[i] + 512, o[2]);
      } else {
        gst8_sc1(dst[i], o[0]);
        gst16_sc1(dst[i] + 508, u32x4{o[1].x, o[1].y, o[2].x, o[2].y});
      }
    }
  } else {
#pragma unroll
    for (int i = 0; i < 2; ++i) {
      const float rs = ms[i].y, mrs = ms[i].x * rs;
      const f32x2 rs2 = f32x2{rs, rs}, nm2 = f32x2{-mrs, -mrs};
      const int toff = (i ^ fh) * 320;
#pragma unroll
      for (int t = 0; t < 4; ++t)
        gst8_sc1(dst[t] + toff, u32x2{pack2v(fma2(lo2(cg[t]), nm2, lo2(acc[i][t]) * rs2)), pack2v(fma2(hi2(cg[t]), nm2, hi2(acc[i][t]) * rs2))});
    }
  }
}

DEV void colsum_gu(const Ctx& c, const u16* wg, const u16* wu, int j, char* csg) {
  const int t = c.tid, ck = t % 24, rg = t / 24;
  float s[8];
#pragma unroll
  for (int e = 0; e < 8; ++e) s[e] = 0.f;
  if (rg < 20) {
    const u16* p = ((ck < 12) ? wg + ck * 8 : wu + (ck - 12) * 8) + 96 * j + (size_t)rg * F_;
#pragma unroll 8
    for (int k = 0; k < 32; ++k) {
      const u32x4 v = *(const u32x4*)(p + (size_t)k * 20 * F_);
      const unsigned v0 = v.x, v1 = v.y, v2 = v.z, v3 = v.w;
      s[0] += bf_lo(v0); s[1] += bf_hi(v0); s[2] += bf_lo(v1); s[3] += bf_hi(v1);
      s[4] += bf_lo(v2); s[5] += bf_hi(v2); s[6] += bf_lo(v3); s[7] += bf_hi(v3);
    }
    LAS float* r = (LAS float*)c.smem + rg * 192 + ck * 8;
    *(LAS f32x4*)r = f32x4{s[0], s[1], s[2], s[3]};
    *(LAS f32x4*)(r + 4) = f32x4{s[4], s[5], s[6], s[7]};
  }
  bar();
  if (t < 48) {
    f32x4 a = f32x4{0.f, 0.f, 0.f, 0.f};
#pragma unroll
    for (int k = 0; k < 20; ++k) a += *(const LAS f32x4*)((LAS float*)c.smem + k * 192 + t * 4);
    gst16_sc1(csg + (j * 192 + t * 4) * 4, __builtin_bit_cast(u32x4, a));
  }
}

template <int KS> struct ECfg {
  static constexpr int KPW = KS / 4;
  static constexpr int CK = (KS == 20) ? 5 : 2;
  static constexpr int NCH = KPW / CK;
  static constexpr int SLOT = 4 * CK * 6 * 1024;
  static constexpr int NS = (KS == 20) ? 1 : 3;
  static constexpr int PPC = 4 * CK * 6 / 8;
  static constexpr int WB0 = (KS == 20) ? 40960 : 0;
};
template <int KS> DEV const u16* e_wsrc(const Ctx& c, const u16* W, int cb) {
  const int kq = c.w >> 1, tg = c.w & 1;
  const int a0 = cb * 160 - (cb & 1) * 32;
  const int r = c.lane >> 2, cp_ = c.lane & 3, cc = cp_ ^ (((r >> 2) & 1) << 1);
  (void)tg;
  const int row = (KS == 20) ? 8 * (r >> 2) + (r & 3) : r;
  return W + (size_t)(kq * ECfg<KS>::KPW * 32 + row) * D_ + (a0 >> 1) + 8 * cc;
}
template <int KS, int J0, int J1> DEV void e_issueW(const Ctx& c, const u16* wsrc, int ch) {
  using C = ECfg<KS>;
  const int kq = c.w >> 1, tg = c.w & 1;
  LAS char* s = c.smem + C::WB0 + (ch % C::NS) * C::SLOT + kq * C::CK * 6 * 1024;
#pragma unroll
  for (int j = J0; j < J1; ++j) {
    const int p0 = 2 * j, p1 = 2 * j + 1;
    constexpr int RG = (KS == 20) ? 4 : 16;
    const int off0 = ((ch * C::CK + p0 / 6) * 32 + ((p0 % 6) / 3) * RG) * D_ + (p0 % 3) * 32;
    const int off1 = ((ch * C::CK + p1 / 6) * 32 + ((p1 % 6) / 3) * RG) * D_ + (p1 % 3) * 32;
    glds16<0, 0>(wsrc + (tg ? off1 : off0), s + (2 * j + tg) * 1024);
  }
}
template <int RM> DEV void e_load_res(const Ctx& c, const u16* res, int rb, int cb, u32x2 (&rv)[5]) {
  const int tt = c.w & 3;
  if (RM) {
    const u16* p = res + (size_t)(rb * 64 + tt * 16 + c.l15) * D_ + cb * 80 + 4 * c.q;
    gld8_sc1<0>(rv[0], p); gld8_sc1<32>(rv[1], p); gld8_sc1<64>(rv[2], p); gld8_sc1<96>(rv[3], p); gld8_sc1<128>(rv[4], p);
  } else {
    const char* p = (const char*)res + ((size_t)(rb * 4 + tt) * 20 * 64 + c.lane) * 16 + ((5 * cb) >> 1) * 1024 + (cb & 1) * 8;
    const char* p2 = p + ((cb & 1) ? 1016 : 8);
    gld8_sc1<0>(rv[0], p); gld8_sc1<0>(rv[1], p2); gld8_sc1<1024>(rv[2], p); gld8_sc1<1024>(rv[3], p2); gld8_sc1<2048>(rv[4], p);
  }
}
template <int KS> struct ESched {
  static constexpr int NU = KS / 2, AW = (KS == 20) ? 10 : 8;
  static constexpr int dchunk(int u) { return (KS == 20) ? -1 : (u % 4 == 3) ? (u - 3) / 4 : (u % 4 == 0 && u >= 4) ? (u - 4) / 4 : (u % 4 == 1 && u >= 5) ? (u - 5) / 4 : -1; }
  static constexpr int dma(int u) { return (dchunk(u) >= 0 && dchunk(u) <= 2) ? 2 : 0; }
  static constexpr int dpart(int u) { return (u % 4 == 3) ? 0 : (u % 4 == 0) ? 2 : 4; }
  static constexpr int aop(int u) { return (u + AW < NU) ? 1 : 0; }
  static constexpr int ops(int u) { return aop(u) + dma(u); }
  static constexpr int wait_act(int u, int nres) {
    int n = 0;
    if (u < AW) { n = (AW - 1 - u) + (u == 0 ? nres : 0); for (int t = 0; t < u; ++t) n += ops(t); }
    else { n = dma(u - AW); for (int t = u - AW + 1; t < u; ++t) n += ops(t); }
    return n;
  }
  static constexpr int wait_w(int u) { int n = 0; for (int t = u - 4; t <= u; ++t) n += ops(t); return n; }
};

template <int KS, int RES_EARLY, int OUT_RM>
DEV void e_main(const Ctx& c, const u16* wsrc, const u16* act, const u16* res, u16* out, char* stp, int rb, int cb, u32x2 (&rv)[5],
                const u64* pollp, bool pollv) {
  using C = ECfg<KS>;
  using E = ESched<KS>;
  constexpr int KPW = C::KPW, CK = C::CK, NU = E::NU, AW = E::AW, RSTRIDE = 336, NRES = RES_EARLY ? 0 : 5;
  const int lane = c.lane, l15 = c.l15, q = c.q, w = c.w;
  const int tt = w & 3, kh = w >> 2;
  LAS char* smem = c.smem;
  const int shift = (cb & 1) * 32;
  u32x4 a[NU];
  const u16* ap0 = act + (((size_t)(rb * 4 + tt) * KS + 2 * kh * KPW) * 64 + lane) * 8;
  const u16* ap1 = ap0 + KPW * 512;
  auto lda = [&](auto U_) {
    constexpr int u = decltype(U_)::value;
    constexpr int ks = (u / (2 * CK)) * CK + (u % CK);
    const u16* p = (((u / CK) & 1) ? ap1 : ap0) + (ks >> 2) * 2048;
    gld16_sc1<(ks & 3) * 1024>(a[u], p);
  };
  int woff[5];
  {
    const int qq = (lane & 15) >> 2, p = lane & 3, r = 4 * q + qq;
#pragma unroll
    for (int ft = 0; ft < 5; ++ft) {
      const int col = shift + 32 * ft, seg = col >> 6, aa = (col >> 5) & 1;
      woff[ft] = C::WB0 + 2 * kh * CK * 6 * 1024 + seg * 1024 + r * 64 + (((2 * aa + (p >> 1)) ^ ((q & 1) << 1)) * 16) + (p & 1) * 8;
    }
  }
#pragma unroll
  for (int t = 0; t < 5; ++t) pin(woff[t]);
  pin(ap0); pin(ap1);
  poll_flags(c, pollp, pollv);
  lda(std::integral_constant<int, 0>{});
  if (!RES_EARLY) e_load_res<0>(c, res, rb, cb, rv);
  static_for<1, AW>([&](auto U_) { lda(U_); });
  f32x4 acc[5];
#pragma unroll
  for (int t = 0; t < 5; ++t) acc[t] = f32x4{0.f, 0.f, 0.f, 0.f};
  auto readw = [&](int u, u32x4 (&wf)[5]) {
    const LAS char* sp = smem + ((u / (2 * CK)) % C::NS) * C::SLOT + (u % (2 * CK)) * 6144;
#pragma unroll
    for (int t = 0; t < 5; ++t) {
      u32x2 g0 = tr_read(sp + woff[t]);
      u32x2 g1 = tr_read(sp + woff[t] + 3072);
      wf[t] = u32x4{g0.x, g0.y, g1.x, g1.y};
    }
  };
  u32x4 wA[5], wB[5];
  wait_vm<AW + NRES>();
  bar();
  readw(0, wA);
  auto step = [&](auto U_, u32x4 (&wc)[5], u32x4 (&wn)[5]) {
    constexpr int u = decltype(U_)::value;
    if (u + 1 < NU) readw(u + 1, wn);
    asm volatile("s_waitcnt vmcnt(%1)" : "+v"(a[u]) : "n"(E::wait_act(u, NRES)) : "memory");
    sched_fence();
    acc[0] = mfma16(wc[0], a[u], acc[0]);
    sched_fence();
    if constexpr (E::aop(u)) lda(std::integral_constant<int, u + AW>{});
    sched_fence();
    acc[1] = mfma16(wc[1], a[u], acc[1]);
    sched_fence();
    if constexpr (E::dma(u)) e_issueW<KS, E::dpart(u), E::dpart(u) + 1>(c, wsrc, E::dchunk(u) + 3);
    sched_fence();
    acc[2] = mfma16(wc[2], a[u], acc[2]);
    acc[3] = mfma16(wc[3], a[u], acc[3]);
    sched_fence();
    if constexpr (E::dma(u)) e_issueW<KS, E::dpart(u) + 1, E::dpart(u) + 2>(c, wsrc, E::dchunk(u) + 3);
    sched_fence();
    acc[4] = mfma16(wc[4], a[u], acc[4]);
    if constexpr (KS != 20 && (u % 4 == 2) && u < 20) {
      if constexpr (u / 4 + 1 >= 3) wait_vm<E::wait_w(u)>();
      bar();
    }
  };
  static_for<0, NU>([&](auto U_) {
    constexpr int u = decltype(U_)::value;
    if (u & 1) step(U_, wB, wA); else step(U_, wA, wB);
  });
  asm volatile("s_waitcnt vmcnt(0)" : "+v"(rv[0]), "+v"(rv[1]), "+v"(rv[2]), "+v"(rv[3]), "+v"(rv[4]) : : "memory");
  bar();
  LAS char* pr = smem + (tt * 16 + l15) * RSTRIDE + 16 * q;
  if (kh == 1) {
#pragma unroll
    for (int t = 0; t < 5; ++t) *(LAS f32x4*)(pr + 64 * t) = acc[t];
  }
  bar();
  if (kh == 0) {
    u32x2 o[5];
    f32x2 sx2 = f32x2{0.f, 0.f}, sq2 = sx2;
#pragma unroll
    for (int ft = 0; ft < 5; ++ft) {
      const f32x4 v = acc[ft] + *(const LAS f32x4*)(pr + 64 * ft);
      const unsigned r0 = rv[ft].x, r1 = rv[ft].y;
      const unsigned o0 = pack2v(f32x2{bf_lo(r0), bf_hi(r0)} + lo2(v)), o1 = pack2v(f32x2{bf_lo(r1), bf_hi(r1)} + hi2(v));
      o[ft] = u32x2{o0, o1};
      if (!OUT_RM) {
        const f32x2 e0 = f32x2{bf_lo(o0), bf_hi(o0)}, e1 = f32x2{bf_lo(o1), bf_hi(o1)};
        sx2 += e0 + e1;
        sq2 = fma2(e0, e0, fma2(e1, e1, sq2));
      }
    }
    float sx = sx2.x + sx2.y, sxx = sq2.x + sq2.y;
    if (OUT_RM) {
      u16* p = out + (size_t)(rb * 64 + tt * 16 + l15) * D_ + cb * 80 + 4 * q;
#pragma unroll
      for (int ft = 0; ft < 5; ++ft) *(u32x2*)(p + 16 * ft) = o[ft];
    } else {
      char* p = (char*)out + ((size_t)(rb * 4 + tt) * 20 * 64 + lane) * 16 + ((5 * cb) >> 1) * 1024;
      if (cb & 1) {
        gst8_sc1(p + 8, o[0]);
        gst16_sc1(p + 1024, u32x4{o[1].x, o[1].y, o[2].x, o[2].y});
        gst16_sc1(p + 2048, u32x4{o[3].x, o[3].y, o[4].x, o[4].y});
      } else {
        gst16_sc1(p, u32x4{o[0].x, o[0].y, o[1].x, o[1].y});
        gst16_sc1(p + 1024, u32x4{o[2].x, o[2].y, o[3].x, o[3].y});
        gst8_sc1(p + 2048, o[4]);
      }
      sx = xsum(sx); sxx = xsum(sxx);
      if (q == 0) gst8_sc1(stp + (((rb * 4 + tt) * 8 + cb) * 16 + l15) * 8, __builtin_bit_cast(u32x2, f32x2{sx, sxx}));
    }
  }
}

DEV float max3(float a, float b, float c_) { float r; asm("v_max3_f32 %0, %1, %2, %3" : "=v"(r) : "v"(a), "v"(b), "v"(c_)); return r; }
DEV void attn_main(const Ctx& c, const u16* QKVt, u16* o_t, int h, int qb, const u64* pollp, bool pollv) {
  LAS char* smem = c.smem;
  const int lane = c.lane, l15 = c.l15, q = c.q, w = c.w;
  const char* kb = (const char*)QKVt + ((ptrdiff_t)(32 + h) * 128 + 16 * qb - 16) * 640 + lane * 16 + w * 1024;
  const int TT0 = 16 * qb + 2 * w;
  const int tsw = l15 ^ (((q >> 1) & 1) << 3);
  const u16* qp = QKVt + ((size_t)h * 128 + TT0) * 320 + l15 * 4;
  const u16* qp2 = QKVt + ((size_t)h * 128 + TT0) * 320 + q * 64 + tsw * 4;
  pin(kb); pin(qp); pin(qp2);
  poll_flags(c, pollp, pollv);
  {
#pragma unroll
    for (int j = 0; j < 5; ++j) {
      const int pc = w + 8 * j;
      const char* src = kb + j * 8192 + ((pc < 20) ? 0 : 32 * 128 * 640 - 20480);
      const bool first_half = (pc < 20) ? (pc < 10) : (pc < 30);
      if (qb == 0 && first_half) *(LAS u32x4*)(smem + pc * 1024 + lane * 16) = u32x4{0, 0, 0, 0};
      else glds16<0, 1>(src, smem + pc * 1024);
    }
  }
  u32x2 ql[2], qh[2];
  gld8_sc1<512>(qh[0], qp); gld8_sc1<640 + 512>(qh[1], qp);
  gld8_sc1<0>(ql[0], qp2); gld8_sc1<640>(ql[1], qp2);
  asm volatile("s_waitcnt vmcnt(0)" : "+v"(ql[0]), "+v"(ql[1]), "+v"(qh[0]), "+v"(qh[1]) : : "memory");
  bar();
  u32x4 qf[2];
#pragma unroll
  for (int i = 0; i < 2; ++i) qf[i] = u32x4{ql[i].x, ql[i].y, q == 0 ? qh[i].x : 0u, q == 0 ? qh[i].y : 0u};
  const LAS char* klo = smem + 2 * w * 640 + q * 128 + tsw * 8;
  const LAS char* khi = smem + 2 * w * 640 + 512 + l15 * 8;
  f32x4 sa[17], sb[17];
  const f32x4 zero4 = f32x4{0.f, 0.f, 0.f, 0.f};
#pragma unroll
  for (int j = 0; j < 18; ++j) {
    u32x2 lo = *(const volatile LAS u32x2*)(klo + j * 640);
    u32x2 hi = *(const volatile LAS u32x2*)(khi + j * 640);
    u32x4 kf = u32x4{lo.x, lo.y, hi.x, hi.y};
    if (j < 17) sa[j] = mfma16(kf, qf[0], zero4);
    if (j > 0) sb[j - 1] = mfma16(kf, qf[1], zero4);
  }
  const float NEG = -1e30f;
#pragma unroll
  for (int r = 0; r < 4; ++r) {
    const bool gt = (4 * q + r) > l15;
    sa[0][r] = gt ? sa[0][r] : NEG; sb[0][r] = gt ? sb[0][r] : NEG;
    sa[16][r] = gt ? NEG : sa[16][r]; sb[16][r] = gt ? NEG : sb[16][r];
  }
  if (qb == 0) {
#pragma unroll
    for (int j = 0; j < 16; ++j) {
      if (2 * w + j < 16) sa[j] = f32x4{NEG, NEG, NEG, NEG};
      if (2 * w + j + 1 < 16) sb[j] = f32x4{NEG, NEG, NEG, NEG};
    }
  }
  float ma = NEG, mbx = NEG;
#pragma unroll
  for (int j = 0; j < 17; ++j) {
    ma = max3(max3(ma, sa[j][0], sa[j][1]), sa[j][2], sa[j][3]);
    mbx = max3(max3(mbx, sb[j][0], sb[j][1]), sb[j][2], sb[j][3]);
  }
  ma = xmax(ma); mbx = xmax(mbx);
  const float C = 0.22360679775f * 1.44269504089f;
  const f32x2 C2 = f32x2{C, C}, na2 = f32x2{-ma * C, -ma * C}, nb2 = f32x2{-mbx * C, -mbx * C};
  auto ex2 = [&](f32x2 s2, f32x2 n2) { const f32x2 e = fma2(s2, C2, n2); return pack2(__builtin_amdgcn_exp2f(e.x), __builtin_amdgcn_exp2f(e.y)); };
  unsigned pa[18][2], pb[18][2];
#pragma unroll
  for (int j = 0; j < 17; ++j) {
    pa[j][0] = ex2(lo2(sa[j]), na2); pa[j][1] = ex2(hi2(sa[j]), na2);
    pb[j + 1][0] = ex2(lo2(sb[j]), nb2); pb[j + 1][1] = ex2(hi2(sb[j]), nb2);
  }
  pa[17][0] = pa[17][1] = 0u; pb[0][0] = pb[0][1] = 0u;
  f32x4 oal = zero4, oah = zero4, obl = zero4, obh = zero4, da = zero4, db = zero4;
  const u32x4 ones = u32x4{0x3f803f80u, 0x3f803f80u, 0x3f803f80u, 0x3f803f80u};
  const int vq = (lane & 15) >> 2, vp = lane & 3, vkey = 4 * q + vq;
  const int dgl = vp + (h & 1), dgh = (h & 1) ? 0 : 4;
  const LAS char* vlo = smem + 20480 + 2 * w * 640 + dgl * 128 + ((vkey ^ (((dgl >> 1) & 1) << 3)) * 8);
  const LAS char* vhi = smem + 20480 + 2 * w * 640 + dgh * 128 + vkey * 8;
#pragma unroll
  for (int u = 0; u < 9; ++u) {
    const u32x2 l0 = tr_read(vlo + (2 * u) * 640), l1 = tr_read(vlo + (2 * u + 1) * 640);
    const u32x2 h0 = tr_read(vhi + (2 * u) * 640), h1 = tr_read(vhi + (2 * u + 1) * 640);
    u32x4 vl = u32x4{l0.x, l0.y, l1.x, l1.y};
    u32x4 vh = u32x4{h0.x, h0.y, h1.x, h1.y};
    u32x4 fa = u32x4{pa[2 * u][0], pa[2 * u][1], pa[2 * u + 1][0], pa[2 * u + 1][1]};
    u32x4 fb = u32x4{pb[2 * u][0], pb[2 * u][1], pb[2 * u + 1][0], pb[2 * u + 1][1]};
    oal = mfma16(vl, fa, oal); oah = mfma16(vh, fa, oah);
    obl = mfma16(vl, fb, obl); obh = mfma16(vh, fb, obh);
    da = mfma16(ones, fa, da); db = mfma16(ones, fb, db);
  }
  const float ia = __builtin_amdgcn_rcpf(da[0]);
  const float ib = __builtin_amdgcn_rcpf(db[0]);
  {
    const unsigned ax = pack2(oal[0] * ia, oal[1] * ia), ay = pack2(oal[2] * ia, oal[3] * ia);
    const unsigned bx = pack2(obl[0] * ib, obl[1] * ib), by = pack2(obl[2] * ib, obl[3] * ib);
    const unsigned hax = pack2(oah[0] * ia, oah[1] * ia), hay = pack2(oah[2] * ia, oah[3] * ia);
    const unsigned hbx = pack2(obh[0] * ib, obh[1] * ib), hby = pack2(obh[2] * ib, obh[3] * ib);
#if defined(__HIP_DEVICE_COMPILE__)
    const auto sx_ = __builtin_amdgcn_permlane16_swap(ax, bx, false, false);
    const auto sy_ = __builtin_amdgcn_permlane16_swap(ay, by, false, false);
    const auto swa_ = __builtin_amdgcn_permlane16_swap(hax, hbx, false, false);
    const auto swb_ = __builtin_amdgcn_permlane16_swap(hay, hby, false, false);
    const unsigned x0 = sx_[0], x1 = sx_[1], y0 = sy_[0], y1 = sy_[1], ta0 = swa_[0], tb0 = swb_[0];
    const int g = h * 5 + (h & 1) + (q & 2);
    const int gh = h * 5 + dgh;
    u16* ot = o_t + (size_t)(TT0 + (q & 1)) * 20 * 512;
    gst16_sc1(ot + (((g >> 3) * 64) + ((g >> 1) & 3) * 16 + l15) * 8, u32x4{x0, y0, x1, y1});
    if (q < 2) gst8_sc1(ot + (((gh >> 3) * 64) + ((gh >> 1) & 3) * 16 + l15) * 8 + (gh & 1) * 4, u32x2{ta0, tb0});
#endif
  }
}

#define Q_RB(c) (4 * ((c).x8 >> 1) + ((c).bi & 3))
#define Q_CB(c) (8 * ((c).x8 & 1) + ((c).bi >> 2))
#define A_H(c) (4 * (c).x8 + ((c).bi & 3))
#define A_QB(c) ((c).bi >> 2)
#define E_RB(c) (8 * ((c).x8 >> 1) + ((c).bi & 7))
#define E_CB(c) (4 * ((c).x8 & 1) + ((c).bi >> 3))
#define G_RB(c) (8 * ((c).x8 >> 2) + ((c).bi & 7))
#define G_CB(c) (4 * ((c).x8 & 3) + ((c).bi >> 3))
template <int L>
DEV void layer(const Ctx& c0, const u16* xin, const u16* wqkv, const u16* wo, const u16* wg, const u16* wu, const u16* wd,
               u16* ws, u16* xout, const u16* qw_pref) {
  const size_t SD = (size_t)S_ * D_;
  u16* base = ws + (size_t)L * (6 * SD + (size_t)S_ * F_);
  u16* QKVt = base;
  u16* o_t = base + 3 * SD; u16* x1 = o_t + SD; u16* h_t = x1 + 2 * SD;
  u64* sy = c0.sync;
  char* aux = (char*)c0.sync;
  char* st_x1 = aux + AUX_ST + (2 * L) * AUX_ST_BYTES;
  char* st_x2 = aux + AUX_ST + (2 * L + 1) * AUX_ST_BYTES;
  {
    const Ctx c = fresh(c0);
    const int q_rb = Q_RB(c), q_cb = Q_CB(c), lane = c.lane, w = c.w;
    if (q_cb < 15) {
      if (L == 0) {
        const u16* wsrc = f_wsrc<0>(c, wqkv, nullptr, q_cb);
        const int r = lane >> 3, cp_ = lane & 7;
        const u16* a0 = xin + (size_t)(q_rb * 128 + 16 * w + r) * D_ + 8 * (cp_ ^ (r >> 1));
        const u16* a1 = xin + (size_t)(q_rb * 128 + 16 * w + 8 + r) * D_ + 8 * (cp_ ^ (4 + (r >> 1)));
#pragma unroll
        for (int ch = 0; ch < 2; ++ch) { f_issueW<0>(c, wsrc, (ch + 1) % 5); f_issueA<0>(c, a0, a1, ch); }
        f_main<0, 0>(c, wsrc, a0, a1, q_rb, q_cb, QKVt, aux, aux + AUX_CSQ, nullptr, false);
      } else {
        f_issueW<1>(c, qw_pref, 4); f_issueW<1>(c, qw_pref, 0);
        const u16* a0 = xin + ((size_t)(q_rb * 8 + w) * 20 * 64 + lane) * 8;
        const u16* a1 = a0;
        f_main<1, 1>(c, qw_pref, a0, a1, q_rb, q_cb, QKVt, aux + AUX_ST + AUX_ST_BYTES, aux + AUX_CSQ,
                     (lane < 16) ? sy + SY_FD + q_rb * 16 + lane : sy + SY_FQ + q_cb, lane < 17);
      }
    } else if (L == 0) {
      colsum_gu(c, wg, wu, (c.x8 >> 1) * 4 + (c.bi & 3), aux + AUX_CSG);
    }
  }
  STAMP(L * 16 + 1);
  {
    const Ctx c = fresh(c0);
    const u16* wsrc = e_wsrc<20>(c, wo, E_CB(c));
    e_issueW<20, 0, 5>(c, wsrc, 0);
    wait_vm<5>();
    bar();
    if (Q_CB(c) < 15) set_flag(c, sy + SY_FQ + L * 256 + Q_RB(c) * 16 + Q_CB(c));
    else if (L == 0) set_flag(c, sy + SY_CS + (c.x8 >> 1) * 4 + (c.bi & 3));
    if (L == 0) set_flag(c, sy + SY_START + c.bi * 8 + c.x8);
    STAMP(L * 16 + 2);
    e_issueW<20, 5, 15>(c, wsrc, 0);
    const int a_h = A_H(c), a_qb = A_QB(c);
    const int i = c.lane, pi = (i * 43) >> 8, which = i - pi * 6;
    const int part = which >> 1, end = which & 1;
    const int cbx = (640 * part + 20 * a_h + 19 * end) >> 7, rbx = 2 * a_qb - 2 + pi;
    const bool valid = (i < 24) && (rbx >= 0) && !(part == 0 && pi < 2);
    attn_main(c, QKVt, o_t, a_h, a_qb, sy + SY_FQ + L * 256 + (valid ? rbx * 16 + cbx : 0), valid);
  }
  STAMP(L * 16 + 4);
  {
    const Ctx c = fresh(c0);
    const int e_rb = E_RB(c), e_cb = E_CB(c);
    u32x2 rv[5];
    if (L == 0) e_load_res<1>(c, xin, e_rb, e_cb, rv);
    if (L == 0) wait_vm<5>(); else wait_vm<0>();
    bar();
    set_flag(c, sy + SY_FA + L * 256 + A_QB(c) * 32 + A_H(c));
    STAMP(L * 16 + 5);
    e_main<20, (L == 0) ? 1 : 0, 0>(c, nullptr, o_t, xin, x1, st_x1, e_rb, e_cb, rv,
                                    sy + SY_FA + L * 256 + (e_rb >> 2) * 32 + (c.lane & 31), c.lane < 32);
  }
  STAMP(L * 16 + 7);
  {
    const Ctx c = fresh(c0);
    const int g_rb = G_RB(c), g_cb = G_CB(c);
    const u16* gw = f_wsrc<2>(c, wg, wu, g_cb);
    f_issueW<2>(c, gw, 1); f_issueW<2>(c, gw, 2);
    wait_vm<6>();
    bar();
    set_flag(c, sy + SY_FW + L * 256 + E_RB(c) * 8 + E_CB(c));
    STAMP(L * 16 + 8);
    f_issueW<2>(c, gw, 3); f_issueW<2>(c, gw, 0);
    const u16* a0 = x1 + ((size_t)(g_rb * 8 + c.w) * 20 * 64 + c.lane) * 8;
    const u16* a1 = a0;
    f_main<2, 1>(c, gw, a0, a1, g_rb, g_cb, h_t, st_x1, aux + AUX_CSG,
                 (c.lane < 16) ? sy + SY_FW + L * 256 + g_rb * 16 + c.lane : sy + SY_CS + g_cb, c.lane < 17);
  }
  STAMP(L * 16 + 10);
  {
    const Ctx c = fresh(c0);
    const int e_rb = E_RB(c), e_cb = E_CB(c);
    const u16* dw = e_wsrc<48>(c, wd, e_cb);
    e_issueW<48, 0, 6>(c, dw, 0);
    wait_vm<6>();
    bar();
    set_flag(c, sy + SY_FG + L * 256 + G_RB(c) * 16 + G_CB(c));
    STAMP(L * 16 + 11);
    e_issueW<48, 0, 6>(c, dw, 1); e_issueW<48, 0, 6>(c, dw, 2);
    u32x2 rv[5];
    e_main<48, 0, L>(c, dw, h_t, x1, xout, st_x2, e_rb, e_cb, rv,
                     sy + SY_FG + L * 256 + (e_rb >> 1) * 16 + (c.lane & 15), c.lane < 16);
  }
  STAMP(L * 16 + 13);
}

__global__ __launch_bounds__(512, 2) void mega(const u16* __restrict__ x, const u16* __restrict__ wqkv, const u16* __restrict__ wo,
                                               const u16* __restrict__ wg, const u16* __restrict__ wu, const u16* __restrict__ wd,
                                               u16* __restrict__ out, u16* __restrict__ ws, u64* __restrict__ sync) {
  __shared__ __attribute__((aligned(16))) char smem_[163840];
  Ctx c;
  c.tid = threadIdx.x; c.lane = c.tid & 63; c.w = __builtin_amdgcn_readfirstlane(c.tid >> 6);
  c.l15 = c.lane & 15; c.q = c.lane >> 4; c.x8 = blockIdx.x & 7; c.bi = blockIdx.x >> 3;
  c.smem = (LAS char*)smem_; c.sync = sync;
  STAMP(40);
  c.tag = 0;
  if (c.w == 0) c.tag = __hip_atomic_load(sync + SY_EPOCH, __ATOMIC_RELAXED, __HIP_MEMORY_SCOPE_AGENT) + 1ull;
  u32x4 wu0, wu1, wu2, wu3;
  {
    const int i = blockIdx.x * 512 + c.tid;
    asm volatile("global_load_dwordx4 %0, %1, off" : "=v"(wu0) : "v"(wd + (size_t)(i < 122880 ? i : 0) * 8) : "memory");
    asm volatile("global_load_dwordx4 %0, %1, off" : "=v"(wu1) : "v"(wo + (size_t)(i < 51200 ? i : 0) * 8) : "memory");
    asm volatile("global_load_dwordx4 %0, %1, off" : "=v"(wu2) : "v"(wg + (size_t)(i < 122880 ? i : 0) * 8) : "memory");
    asm volatile("global_load_dwordx4 %0, %1, off" : "=v"(wu3) : "v"(wu + (size_t)(i < 122880 ? i : 0) * 8) : "memory");
  }
  const size_t SD = (size_t)S_ * D_;
  u16* x2 = ws + 5 * SD;
  layer<0>(c, x, wqkv, wo, wg, wu, wd, ws, x2, nullptr);
  asm volatile("" : "+v"(wu0), "+v"(wu1), "+v"(wu2), "+v"(wu3));
  c = fresh(c);
  const int q_cb = Q_CB(c);
  const u16* qw = f_wsrc<1>(c, wqkv, nullptr, q_cb < 15 ? q_cb : 0);
  if (q_cb < 15) {
    f_issueW<1>(c, qw, 1); f_issueW<1>(c, qw, 2); f_issueW<1>(c, qw, 3);
    wait_vm<6>();
  } else wait_vm<0>();
  bar();
  set_flag(c, sync + SY_FD + E_RB(c) * 8 + E_CB(c));
  STAMP(14);
  layer<1>(c, x2, wqkv, wo, wg, wu, wd, ws, out, qw);
  c = fresh(c);
  if (blockIdx.x == 0 && c.w == 0) {
    unsigned spins = 0;
    while (true) {
      bool ok = true;
#pragma unroll
      for (int k = 0; k < 4; ++k)
        ok &= (__hip_atomic_load(sync + SY_START + k * 64 + c.lane, __ATOMIC_RELAXED, __HIP_MEMORY_SCOPE_AGENT) == c.tag);
      if (__all(ok)) break;
      __builtin_amdgcn_s_sleep(1);
      if (++spins > SPIN_CAP) break;
    }
    if (c.lane == 0) __hip_atomic_store(sync + SY_EPOCH, c.tag, __ATOMIC_RELAXED, __HIP_MEMORY_SCOPE_AGENT);
  }
  STAMP(41);
}

extern "C" void kernel_launch(void* const* d_in, const int* in_sizes, int n_in, void* d_out, int out_size,
                              void* d_ws, size_t ws_size, hipStream_t stream) {
  u64* sync = (u64*)d_ws;
  u16* ws = (u16*)((char*)d_ws + WS_ACT);
  mega<<<256, 512, 0, stream>>>((const u16*)d_in[0], (const u16*)d_in[1], (const u16*)d_in[2], (const u16*)d_in[4],
                                (const u16*)d_in[5], (const u16*)d_in[6], (u16*)d_out, ws, sync);
}
```
